```python
import torch

def _softmax(x, axis=-1):
    return torch.softmax(x, dim=axis)

def _gelu(x):
    return torch.nn.functional.gelu(x, approximate='tanh')

def _silu(x):
    return torch.nn.functional.silu(x)

def _sigmoid(x):
    return torch.sigmoid(x)

def _layernorm(x, axis=-1, eps=1e-05):
    m = x.mean(dim=axis, keepdim=True)
    v = x.var(dim=axis, keepdim=True, unbiased=False)
    return (x - m) / torch.sqrt(v + eps)

def _scatter_add(tgt, idx, src, axis=0):
    return tgt.clone().index_add_(axis, idx, src)

def _segment_sum(src, seg, n):
    out = torch.zeros((n,) + src.shape[1:], dtype=src.dtype)
    return out.index_add_(0, seg, src)

def _pad(x, pw):
    flat = tuple((v for pair in reversed(pw) for v in pair))
    return torch.nn.functional.pad(x, flat)
kernel_name = 'adaLN_cond__memory_aug_recurrent_mem_tr__mixer_channel_mlp__prompt'

def setup_inputs():

    _w = lambda a: (lambda t: t.to(torch.bfloat16) if t.is_floating_point() else t)(torch.as_tensor(a))

    def _A_setup():
        B, S, Dm = (1, 2048, 512)
        H, Dh = (32, 128)
        M, seg = (8, 32)
        seg = min(seg, S)
        S = S // seg * seg
        return {'x': _w((lambda *s: torch.randn(*s, dtype=torch.bfloat16) * 0.05)(B, S, Dm)), 'mem0': _w((lambda *s: torch.randn(*s, dtype=torch.bfloat16) * 0.4)(B, M, Dm)), 'Wq': _w((lambda *s: torch.randn(*s, dtype=torch.bfloat16) * 3.2)(Dm, H, Dh) * Dm ** (-0.5)), 'Wk': _w((lambda *s: torch.randn(*s, dtype=torch.bfloat16) * 3.2)(Dm, H, Dh) * Dm ** (-0.5)), 'Wv': _w((lambda *s: torch.randn(*s, dtype=torch.bfloat16) * 0.4)(Dm, H, Dh) * Dm ** (-0.5)), 'Wo': _w((lambda *s: torch.randn(*s, dtype=torch.bfloat16) * 0.4)(H, Dh, Dm) * (H * Dh) ** (-0.5)), 'seg_len': seg}
    _A_in = _A_setup()

    def _B_setup():
        B, S, D, F = (1, 2048, 512, 1792)
        return {'x': _w((lambda *s: torch.randn(*s, dtype=torch.bfloat16) * 0.05)(B, S, D)), 'w1': _w((lambda *s: torch.randn(*s, dtype=torch.bfloat16) * 3.2)(D, F) * D ** (-0.5)), 'b1': _w((lambda *s: torch.randn(*s, dtype=torch.bfloat16) * 0.1)(F)), 'w2': _w((lambda *s: torch.randn(*s, dtype=torch.bfloat16) * 0.1)(F, D) * F ** (-0.5)), 'b2': _w((lambda *s: torch.randn(*s, dtype=torch.bfloat16) * 0.1)(D))}
    _B_in = _B_setup()
    return {'A_x': _A_in['x'], 'A_mem0': _A_in['mem0'], 'A_Wq': _A_in['Wq'], 'A_Wk': _A_in['Wk'], 'A_Wv': _A_in['Wv'], 'A_Wo': _A_in['Wo'], 'A_seg_len': _A_in['seg_len'], 'B_x': _B_in['x'], 'B_w1': _B_in['w1'], 'B_b1': _B_in['b1'], 'B_w2': _B_in['w2'], 'B_b2': _B_in['b2'], 'c': _w((lambda *s: torch.randn(*s, dtype=torch.bfloat16) * 0.05)(1, 512) * 8.0), 'w_mod': _w((lambda *s: torch.randn(*s, dtype=torch.bfloat16) * 0.05)(512, 6 * 512) * 512 ** (-0.5) * 8.0)}

def reference(A_x, A_mem0, A_Wq, A_Wk, A_Wv, A_Wo, A_seg_len, B_x, B_w1, B_b1, B_w2, B_b2, c, w_mod):

    def _A_ref(x, mem0, Wq, Wk, Wv, Wo, seg_len):
        B, S, Dm = x.shape
        H, Dh = (Wq.shape[1], Wq.shape[2])
        M = mem0.shape[1]
        scale = Dh ** (-0.5)
        n_seg = S // seg_len
        L = M + seg_len
        idx = torch.arange(L)
        mask = (idx[:, None] < M) | (idx[:, None] >= idx[None, :])
        mem = mem0
        outs = []
        for s in range(n_seg):
            xs = x[:, s * seg_len:(s + 1) * seg_len, :].clone()
            h = torch.concatenate([mem, xs], axis=1)
            q = torch.einsum('btd,dhe->bhte', h, Wq)
            k = torch.einsum('btd,dhe->bhte', h, Wk)
            v = torch.einsum('btd,dhe->bhte', h, Wv)
            scores = torch.einsum('bhqe,bhke->bhqk', q, k) * scale
            scores = torch.where(mask[None, None], scores, -1000000000.0)
            p = _softmax(scores, axis=-1)
            o = torch.einsum('bhqk,bhke->bhqe', p, v)
            o = torch.einsum('bhte,hed->btd', o, Wo)
            mem = o[:, :M, :].clone()
            outs.append(o[:, M:, :])
        return torch.concatenate(outs, axis=1)

    def _B_ref(x, w1, b1, w2, b2):
        y = _layernorm(x, axis=-1)
        h = _gelu(torch.einsum('bsd,df->bsf', y, w1) + b1)
        return torch.einsum('bsf,fd->bsd', h, w2) + b2
    x = A_x
    mod = (_silu(c) @ w_mod).reshape(c.shape[0], 6, 1, -1)
    sa, ca, ga, sb, cb, gb = (mod[:, 0].clone(), mod[:, 1].clone(), mod[:, 2].clone(), mod[:, 3].clone(), mod[:, 4].clone(), mod[:, 5].clone())
    h = _layernorm(x, axis=-1) * (1 + ca) + sa
    x = x + ga * _A_ref(h, A_mem0, A_Wq, A_Wk, A_Wv, A_Wo, A_seg_len)
    h = _layernorm(x, axis=-1) * (1 + cb) + sb
    x = x + gb * _B_ref(h, B_w1, B_b1, B_w2, B_b2)
    return x
```

```cpp
constexpr int HL_MEMA = 0, HL_QT = HL_MEMA + 8192, HL_KT = HL_QT + 2048, HL_OA = HL_KT + 2048, HL_KTOK = HL_OA + 2048, HL_WV = HL_KTOK + 8192, HL_END = HL_WV + 131072;
#define SPIN_CAP (1u << 22)

DEV v4u lds_rd4(const char* smem, int off) { return *(const v4u*)(smem + off); }
DEV v2u lds_tr(const char* smem, int off) {
    typedef short v4i16_t __attribute__((ext_vector_type(4)));
    return __builtin_bit_cast(v2u, __builtin_amdgcn_ds_read_tr16_b64_v4i16((LAS v4i16_t*)(smem + off)));
}
DEV float dot2self(unsigned w, float acc) { float a = lo16f(w), b = hi16f(w); return fmaf(a, a, fmaf(b, b, acc)); }

template <bool MEGA>
DEV void head_main(const Ptrs& p, int h, char* smem) {
    const int tid = threadIdx.x, lane = tid & 63, wave = __builtin_amdgcn_readfirstlane(tid >> 6);
    const int g = lane >> 4, l15 = lane & 15;
    __amdgpu_buffer_rsrc_t rs = mkrs(p.ws);
    constexpr int AX = MEGA ? 16 : 0;
    v8u WQ[2][8], WK[2][8], WO[8][2];
#pragma unroll
    for (int i = 0; i < 2; ++i)
#pragma unroll
        for (int t = 0; t < 8; ++t) {
            unsigned oq = (unsigned)(WS_WQKV + (size_t)(h * 3 + 0) * 131072 + (2 * t) * 8192 + (2 * wave + i) * 1024 + lane * 16);
            v4u x0 = bld4<0>(rs, oq), x1 = bld4<0>(rs, oq + 8192);
            WQ[i][t] = (v8u){x0.x, x0.y, x0.z, x0.w, x1.x, x1.y, x1.z, x1.w};
            v4u y0 = bld4<0>(rs, oq + 131072), y1 = bld4<0>(rs, oq + 131072 + 8192);
            WK[i][t] = (v8u){y0.x, y0.y, y0.z, y0.w, y1.x, y1.y, y1.z, y1.w};
            v4u z0 = bld4<0>(rs, oq + 262144), z1 = bld4<0>(rs, oq + 262144 + 8192);
            *(v4u*)(smem + HL_WV + (((wave * 2 + i) * 8 + t) * 2 + 0) * 1024 + lane * 16) = z0;
            *(v4u*)(smem + HL_WV + (((wave * 2 + i) * 8 + t) * 2 + 1) * 1024 + lane * 16) = z1;
        }
#pragma unroll
    for (int n = 0; n < 8; ++n)
#pragma unroll
        for (int t = 0; t < 2; ++t) {
            unsigned oo = (unsigned)(WS_WO + (size_t)(h * 4 + 2 * t) * 32768 + (wave * 8 + n) * 1024 + lane * 16);
            v4u x0 = bld4<0>(rs, oo), x1 = bld4<0>(rs, oo + 32768);
            WO[n][t] = (v8u){x0.x, x0.y, x0.z, x0.w, x1.x, x1.y, x1.z, x1.w};
        }
    for (int idx = tid; idx < 2048; idx += 256) {
        int m = idx >> 8, c = (idx & 255) * 2;
        unsigned w = *(const unsigned*)(p.mem0 + m * 512 + c);
        *(unsigned*)(smem + HL_MEMA + (c >> 6) * 1024 + (m + 8 * ((c >> 1) & 1) + 16 * ((c >> 4) & 3)) * 16 + ((c >> 2) & 3) * 4) = w;
    }
    const unsigned* ctl = (const unsigned*)(p.ws + WS_CTL);
    const float hmax = __uint_as_float(ctl[CTL_HMAX]);
    const float cw = 1.6f * __uint_as_float(ctl[CTL_FV2]) * __uint_as_float(ctl[CTL_CM2]);
    const int sidx = (l15 < 8) ? 0x4444 : 0xEEEE;
    v4u VT[2];
    {
        const unsigned pr = (unsigned)h;
        v4u k0 = bld4<AX>(rs, (unsigned)WS_K + pr * 8192 + tid * 16), k1 = bld4<AX>(rs, (unsigned)WS_K + pr * 8192 + 4096 + tid * 16);
        *(v4u*)(smem + HL_KTOK + tid * 16) = k0; *(v4u*)(smem + HL_KTOK + 4096 + tid * 16) = k1;
        VT[0] = bld4<AX>(rs, (unsigned)WS_V + pr * 8192 + (2 * wave) * 1024 + lane * 16);
        VT[1] = bld4<AX>(rs, (unsigned)WS_V + pr * 8192 + (2 * wave + 1) * 1024 + lane * 16);
    }
    __syncthreads();
    const float sc = 0.08838834764831845f * 1.4426950408889634f;
    const int trq = ((l15 >> 2) + 8 * g) * 16 + (lane & 1) * 8;
    for (int s = 0; s < NSEG; ++s) {
        v4f aq0 = {0, 0, 0, 0}, aq1 = aq0, ak0 = aq0, ak1 = aq0;
#pragma unroll
        for (int t = 0; t < 8; ++t) {
            v4u a = lds_rd4(smem, HL_MEMA + t * 1024 + lane * 16);
            aq0 = smfmac(a, WQ[0][t], aq0, sidx); aq1 = smfmac(a, WQ[1][t], aq1, sidx);
            ak0 = smfmac(a, WK[0][t], ak0, sidx); ak1 = smfmac(a, WK[1][t], ak1, sidx);
        }
        {
            v2u oq, ok;
            oq.x = pack2(fold2(aq0[0], aq1[0]), fold2(aq0[1], aq1[1])); oq.y = pack2(fold2(aq0[2], aq1[2]), fold2(aq0[3], aq1[3]));
            ok.x = pack2(fold2(ak0[0], ak1[0]), fold2(ak0[1], ak1[1])); ok.y = pack2(fold2(ak0[2], ak1[2]), fold2(ak0[3], ak1[3]));
            const int e = 32 * wave + 16 * (lane >> 5) + l15;
            *(v2u*)(smem + HL_QT + e * 16 + (g & 1) * 8) = oq;
            *(v2u*)(smem + HL_KT + e * 16 + (g & 1) * 8) = ok;
        }
        __syncthreads();
        v4f av0 = {0, 0, 0, 0}, av1 = av0;
        v4f an = {0, 0, 0, 0};
        v4f sm = {0, 0, 0, 0}, s0 = sm, s1 = sm;
        v4u kmw;
#pragma unroll
        for (int ks = 0; ks < 4; ++ks) {
            v2u qa = lds_tr(smem, HL_QT + ks * 512 + trq), qb = lds_tr(smem, HL_QT + ks * 512 + 64 + trq);
            v2u ka = lds_tr(smem, HL_KT + ks * 512 + trq), kb = lds_tr(smem, HL_KT + ks * 512 + 64 + trq);
            v4u qf = {qa.x, qa.y, qb.x, qb.y}, kf = {ka.x, ka.y, kb.x, kb.y};
            if (ks == wave) kmw = kf;
            v4u t0 = lds_rd4(smem, HL_KTOK + ks * 1024 + lane * 16), t1 = lds_rd4(smem, HL_KTOK + 4096 + ks * 1024 + lane * 16);
            sm = mfma16(kf, qf, sm); s0 = mfma16(t0, qf, s0); s1 = mfma16(t1, qf, s1);
            if (ks & 1) __builtin_amdgcn_sched_barrier(0);
        }
#pragma unroll
        for (int t = 0; t < 8; ++t) {
            v4u a = lds_rd4(smem, HL_MEMA + t * 1024 + lane * 16);
            v4u x0 = lds_rd4(smem, HL_WV + (((wave * 2 + 0) * 8 + t) * 2) * 1024 + lane * 16), x1 = lds_rd4(smem, HL_WV + (((wave * 2 + 0) * 8 + t) * 2 + 1) * 1024 + lane * 16);
            v4u y0 = lds_rd4(smem, HL_WV + (((wave * 2 + 1) * 8 + t) * 2) * 1024 + lane * 16), y1 = lds_rd4(smem, HL_WV + (((wave * 2 + 1) * 8 + t) * 2 + 1) * 1024 + lane * 16);
            av0 = smfmac(a, (v8u){x0.x, x0.y, x0.z, x0.w, x1.x, x1.y, x1.z, x1.w}, av0, sidx);
            av1 = smfmac(a, (v8u){y0.x, y0.y, y0.z, y0.w, y1.x, y1.y, y1.z, y1.w}, av1, sidx);
            an = mfma16(a, a, an);
            if (t & 1) __builtin_amdgcn_sched_barrier(0);
        }
        v2u vp;
        vp.x = pack2(fold2(av0[0], av1[0]), fold2(av0[1], av1[1])); vp.y = pack2(fold2(av0[2], av1[2]), fold2(av0[3], av1[3]));
        bst4<AX>(rs, (unsigned)(WS_KM + (size_t)(s * 32 + h) * 4096 + wave * 1024 + lane * 16), kmw);
        bst2<AX>(rs, (unsigned)(WS_VM + (size_t)(s * 32 + h) * 2048 + wave * 512 + lane * 8), vp);
        if (s == NSEG - 1) break;
        float mx = -3.0e38f;
#pragma unroll
        for (int r = 0; r < 4; ++r) { sm[r] *= sc; s0[r] *= sc; s1[r] *= sc; mx = fmaxf(mx, fmaxf(sm[r], fmaxf(s0[r], s1[r]))); }
        mx = xmax16_32(mx);
        float sum = 0.f;
#pragma unroll
        for (int r = 0; r < 4; ++r) {
            sm[r] = __builtin_amdgcn_exp2f(sm[r] - mx); s0[r] = __builtin_amdgcn_exp2f(s0[r] - mx); s1[r] = __builtin_amdgcn_exp2f(s1[r] - mx);
            sum += (g < 2 ? sm[r] : 0.f) + s0[r] + s1[r];
        }
        sum = xsum16_32(sum);
        const float inv = 1.f / sum;
        v4u p0, p1;
        p0.x = pack2(sm[0] * inv, sm[1] * inv); p0.y = pack2(sm[2] * inv, sm[3] * inv); p0.z = 0; p0.w = 0;
        p1.x = pack2(s0[0] * inv, s0[1] * inv); p1.y = pack2(s0[2] * inv, s0[3] * inv);
        p1.z = pack2(s1[0] * inv, s1[1] * inv); p1.w = pack2(s1[2] * inv, s1[3] * inv);
        {
            v4u b0 = {lane < 32 ? vp.x : 0u, lane < 32 ? vp.y : 0u, 0, 0}, b1 = {lane < 32 ? 0u : vp.x, lane < 32 ? 0u : vp.y, 0, 0};
            v4f o0 = {0, 0, 0, 0}, o1 = o0;
            o0 = mfma16(p0, b0, o0); o1 = mfma16(p0, b1, o1);
            o0 = mfma16(p1, VT[0], o0); o1 = mfma16(p1, VT[1], o1);
            const int i = lane >> 5;
            const int c = (32 * wave + 16 * i + l15) & 63;
            char* dst = smem + HL_OA + (wave >> 1) * 1024 + (4 * (g & 1) + 8 * ((c >> 1) & 1) + 16 * (c >> 4)) * 16 + ((c >> 2) & 3) * 4 + (c & 1) * 2;
#pragma unroll
            for (int r = 0; r < 4; ++r) *(bf16_t*)(dst + r * 16) = (bf16_t)(i ? o1[r] : o0[r]);
        }
        float nrm = (l15 & 2) ? ((l15 & 1) ? an[3] : an[2]) : ((l15 & 1) ? an[1] : an[0]);
        nrm = wave_sum(((l15 >> 2) == g) ? nrm : 0.f);
        float Bv = sqrtf(cw * fmaxf(hmax, nrm));
        if (!(nrm == nrm)) Bv = nrm;
        int eB = (int)((__float_as_uint(Bv) >> 23) & 255) + 1 - 127;
        eB = eB < -90 ? -90 : eB;
        const float scale = __uint_as_float((unsigned)(127 + 25 - eB) << 23);
        float inv64 = __uint_as_float((unsigned)(127 - 25 - 6 + eB) << 23);
        if (eB > 120) inv64 = __uint_as_float(0x7fc00000u);
        __syncthreads();
        v4u kn0, kn1;
        {
            const unsigned pr = (unsigned)((s + 1) * 32 + h);
            kn0 = bld4<AX>(rs, (unsigned)WS_K + pr * 8192 + tid * 16); kn1 = bld4<AX>(rs, (unsigned)WS_K + pr * 8192 + 4096 + tid * 16);
            VT[0] = bld4<AX>(rs, (unsigned)WS_V + pr * 8192 + (2 * wave) * 1024 + lane * 16);
            VT[1] = bld4<AX>(rs, (unsigned)WS_V + pr * 8192 + (2 * wave + 1) * 1024 + lane * 16);
        }
        {
            v4u a0 = lds_rd4(smem, HL_OA + lane * 16), a1 = lds_rd4(smem, HL_OA + 1024 + lane * 16);
            GAS unsigned* accp = (GAS unsigned*)(p.ws + WS_ACC) + (size_t)s * 4096 + wave * 1024 + lane;
#pragma unroll
            for (int i = 0; i < 4; ++i) {
                v4f c0 = {0, 0, 0, 0}, c1 = c0;
                c0 = smfmac(a0, WO[2 * i][0], c0, sidx); c1 = smfmac(a0, WO[2 * i + 1][0], c1, sidx);
                c0 = smfmac(a1, WO[2 * i][1], c0, sidx); c1 = smfmac(a1, WO[2 * i + 1][1], c1, sidx);
#pragma unroll
                for (int r = 0; r < 4; ++r) {
                    int v = (int)__builtin_rintf(fold2(c0[r], c1[r]) * scale);
                    __hip_atomic_fetch_add(accp + i * 256 + r * 64, ((unsigned)v << 6) | 1u, RLX_AGENT);
                }
            }
        }
        *(v4u*)(smem + HL_KTOK + tid * 16) = kn0; *(v4u*)(smem + HL_KTOK + 4096 + tid * 16) = kn1;
        asm volatile("s_waitcnt vmcnt(0)" ::: "memory");
        {
            v4u r[4];
            unsigned spins = 0;
            const unsigned aoff = (unsigned)(WS_ACC + (size_t)s * 16384 + tid * 16);
            for (;;) {
                asm volatile("" ::: "memory");
#pragma unroll
                for (int i = 0; i < 4; ++i) r[i] = bld4<16>(rs, aoff + i * 4096);
                unsigned bad = 0;
#pragma unroll
                for (int i = 0; i < 4; ++i) bad |= (r[i].x ^ 32u) | (r[i].y ^ 32u) | (r[i].z ^ 32u) | (r[i].w ^ 32u);
                if (!__any((bad & 63u) != 0) || ++spins > SPIN_CAP) break;
            }
            const float off = -32.f * inv64;
#pragma unroll
            for (int i = 0; i < 4; ++i) {
                const int grp = i * 4 + wave, rr = (lane >> 4), unit = lane & 15;
                const int tile = 2 * grp + (unit >> 3), m = 4 * ((unit >> 2) & 1) + rr;
                char* dst = smem + HL_MEMA + (tile >> 2) * 1024 + (m + 16 * (tile & 3)) * 16 + (unit & 3) * 4;
                float f0 = fmaf((float)(int)r[i].x, inv64, off), f1 = fmaf((float)(int)r[i].y, inv64, off);
                float f2 = fmaf((float)(int)r[i].z, inv64, off), f3 = fmaf((float)(int)r[i].w, inv64, off);
                *(unsigned*)dst = pack2(f0, f1);
                *(unsigned*)(dst + 128) = pack2(f2, f3);
            }
        }
        __syncthreads();
    }
}

__global__ void __launch_bounds__(256, 1) k_qkv(Ptrs p) {
    const int wave = __builtin_amdgcn_readfirstlane(threadIdx.x >> 6), lane = threadIdx.x & 63;
    task_qkv<0>(p, blockIdx.x >> 5, blockIdx.x & 31, wave, lane);
}
__global__ void __launch_bounds__(256, 1) k_head(Ptrs p) {
    extern __shared__ __attribute__((aligned(16))) char smem[];
    head_main<false>(p, blockIdx.x, smem);
}
__global__ void __launch_bounds__(256) k_att(Ptrs p) {
    const int wave = __builtin_amdgcn_readfirstlane(threadIdx.x >> 6), lane = threadIdx.x & 63;
    int pair = blockIdx.x * 4 + wave;
    att_pair<0, 0>(p, pair >> 5, pair & 31, lane);
}
__global__ void __launch_bounds__(256) k_wo(Ptrs p) {
    const int wave = __builtin_amdgcn_readfirstlane(threadIdx.x >> 6), lane = threadIdx.x & 63;
    task_wo<2, 2, 0, 0>(p, (blockIdx.x >> 2) * 2, (blockIdx.x & 3) * 8 + wave * 2, lane);
}
__global__ void __launch_bounds__(256) k_ln2(Ptrs p) {
    const int wave = threadIdx.x >> 6, lane = threadIdx.x & 63;
    const int row = blockIdx.x * 4 + wave;
    v4u y = ln2_row<0>(p, row, lane);
    *(v4u*)(p.ws + WS_H + ((size_t)(row >> 4) * 16 + (lane >> 2)) * 1024 + ((row & 15) + 16 * (lane & 3)) * 16) = y;
}
__global__ void __launch_bounds__(256) k_mlp1(Ptrs p) {
    const int wave = __builtin_amdgcn_readfirstlane(threadIdx.x >> 6), lane = threadIdx.x & 63;
    task_mlp1<4, 4, 0, 0>(p, WS_H, (blockIdx.x / 7) * 4, (blockIdx.x % 7) * 16 + wave * 4, lane);
}
__global__ void __launch_bounds__(256) k_mlp2(Ptrs p) {
    const int wave = __builtin_amdgcn_readfirstlane(threadIdx.x >> 6), lane = threadIdx.x & 63;
    task_mlp2<2, 2, 0>(p, (blockIdx.x >> 2) * 2, (blockIdx.x & 3) * 8 + wave * 2, lane);
}

extern "C" void kernel_launch(void* const* d_in, const int* in_sizes, int n_in, void* d_out, int out_size, void* d_ws, size_t ws_size, hipStream_t stream) {
    Ptrs p;
    p.x = (const bf16_t*)d_in[0]; p.mem0 = (const bf16_t*)d_in[1]; p.wq = (const bf16_t*)d_in[2]; p.wk = (const bf16_t*)d_in[3];
    p.wv = (const bf16_t*)d_in[4]; p.wo = (const bf16_t*)d_in[5]; p.w1 = (const bf16_t*)d_in[8]; p.b1 = (const bf16_t*)d_in[9];
    p.w2 = (const bf16_t*)d_in[10]; p.b2 = (const bf16_t*)d_in[11]; p.c = (const bf16_t*)d_in[12]; p.wmod = (const bf16_t*)d_in[13];
    p.ws = (char*)d_ws; p.out = (bf16_t*)d_out;
    (void)hipFuncSetAttribute((const void*)k_head, hipFuncAttributeMaxDynamicSharedMemorySize, HL_END);
    k_prep<<<PREP_BLOCKS, 256, 0, stream>>>(p);
    k_ln1<<<513, 256, 0, stream>>>(p);
    k_qkv<<<512, 256, 0, stream>>>(p);
    k_head<<<32, 256, HL_END, stream>>>(p);
    k_att<<<512, 256, 0, stream>>>(p);
    k_wo<<<256, 256, 0, stream>>>(p);
    k_ln2<<<512, 256, 0, stream>>>(p);
    k_mlp1<<<224, 256, 0, stream>>>(p);
    k_mlp2<<<256, 256, 0, stream>>>(p);
}
```

```cpp
constexpr int CTL_QKVF = 1024;
constexpr int CTL_KVM  = 2048;
constexpr int CTL_ATTC = 4096;
constexpr int CTL_WOC  = CTL_ATTC + 1024;
constexpr int CTL_M1C  = CTL_WOC + 1024;
constexpr int NWORK = 224, WGRP = 56;

DEV unsigned ld_ctl(const Ptrs& p, int idx) { return __hip_atomic_load((GAS unsigned*)(p.ws + WS_CTL) + idx, RLX_AGENT); }
DEV void st_ctl(const Ptrs& p, int idx, unsigned v) { __hip_atomic_store((GAS unsigned*)(p.ws + WS_CTL) + idx, v, RLX_AGENT); }
DEV void add_ctl(const Ptrs& p, int idx) { __hip_atomic_fetch_add((GAS unsigned*)(p.ws + WS_CTL) + idx, 1u, RLX_AGENT); }
#define BAR_ONLY() asm volatile("s_waitcnt lgkmcnt(0)\n\ts_barrier" ::: "memory")
#define DRAIN() asm volatile("s_waitcnt vmcnt(0)" ::: "memory")
DEV void nap(bool fast) { if (fast) __builtin_amdgcn_s_sleep(6); else __builtin_amdgcn_s_sleep(40); }
DEV void wait_counter(const Ptrs& p, int idx, unsigned want, int wave, bool fast) {
    if (wave == 0) {
        unsigned spins = 0;
        while ((unsigned)__builtin_amdgcn_readfirstlane(ld_ctl(p, idx)) < want && ++spins < SPIN_CAP) nap(fast);
    }
    BAR_ONLY();
}
DEV void signal_counter(const Ptrs& p, int idx) {
    DRAIN();
    BAR_ONLY();
    if (threadIdx.x == 0) add_ctl(p, idx);
}

DEV void worker_att(const Ptrs& p, int grp, int slot, int wave, int lane) {
    const int h = slot * 4 + wave;
    for (int seg = grp; seg < NSEG; seg += 4) {
        unsigned spins = 0;
        while ((__builtin_amdgcn_readfirstlane(ld_ctl(p, CTL_QKVF + (seg >> 2) * 32 + h)) == 0 || __builtin_amdgcn_readfirstlane(ld_ctl(p, CTL_KVM + seg * 32 + h)) == 0) && ++spins < SPIN_CAP)
            nap(seg >= NSEG - 4);
        asm volatile("" ::: "memory");
        att_pair<16, 16>(p, seg, h, lane);
        signal_counter(p, CTL_ATTC + seg * 16);
    }
}

DEV v4f ksplit_reduce(v4f (&acc)[2][2], char* smem, int wave, int lane) {
#pragma unroll
    for (int m = 0; m < 2; ++m)
#pragma unroll
        for (int n = 0; n < 2; ++n) *(v4f*)(smem + ((wave * 4 + m * 2 + n) * 64 + lane) * 16) = acc[m][n];
    BAR_ONLY();
    v4f r = *(const v4f*)(smem + ((0 * 4 + wave) * 64 + lane) * 16);
#pragma unroll
    for (int w = 1; w < 4; ++w) r += *(const v4f*)(smem + ((w * 4 + wave) * 64 + lane) * 16);
    return r;
}

DEV void worker_wo(const Ptrs& p, int grp, int j, int wave, int lane, char* smem) {
    __amdgpu_buffer_rsrc_t rs = mkrs(p.ws);
    v4u B[32][2];
#pragma unroll
    for (int ks = 0; ks < 32; ++ks)
#pragma unroll
        for (int n = 0; n < 2; ++n) B[ks][n] = bld4<0>(rs, (unsigned)(WS_WO + (size_t)(wave * 32 + ((ks + 2 * j) & 31)) * 32768 + (2 * j + n) * 1024 + lane * 16));
    const int g = lane >> 4, l15 = lane & 15;
    const int c = (2 * j + (wave & 1)) * 16 + 4 * g;
    const v4f ga = *(const v4f*)((const float*)(p.ws + WS_MOD) + 1024 + c);
    for (int seg = grp; seg < NSEG; seg += 4) {
        const int row = (seg * 2 + (wave >> 1)) * 16 + l15;
        const v2u xv = *(const v2u*)(p.x + (size_t)row * 512 + c);
        wait_counter(p, CTL_ATTC + seg * 16, 8, wave, seg >= NSEG - 4);
        v4f acc[2][2] = {{{0, 0, 0, 0}, {0, 0, 0, 0}}, {{0, 0, 0, 0}, {0, 0, 0, 0}}};
        const unsigned aoff = (unsigned)(WS_O + (size_t)seg * 2 * 131072 + wave * 32768 + lane * 16);
#pragma unroll
        for (int kb = 0; kb < 32; kb += 16) {
            v4u A[16][2];
#pragma unroll
            for (int k = 0; k < 16; ++k) { const unsigned ko = (unsigned)((kb + k + 2 * j) & 31) * 1024; A[k][0] = bld4<16>(rs, aoff + ko); A[k][1] = bld4<16>(rs, aoff + 131072 + ko); }
#pragma unroll
            for (int k = 0; k < 16; ++k)
#pragma unroll
                for (int m = 0; m < 2; ++m)
#pragma unroll
                    for (int n = 0; n < 2; ++n) acc[m][n] = mfma16(B[kb + k][n], A[k][m], acc[m][n]);
        }
        v4f r = ksplit_reduce(acc, smem, wave, lane);
        float r0 = fmaf(ga[0], r[0], lo16f(xv.x)), r1 = fmaf(ga[1], r[1], hi16f(xv.x));
        float r2 = fmaf(ga[2], r[2], lo16f(xv.y)), r3 = fmaf(ga[3], r[3], hi16f(xv.y));
        v2u o; o.x = pack2(r0, r1); o.y = pack2(r2, r3);
        bst2<16>(rs, (unsigned)(WS_X1 + ((size_t)row * 512 + c) * 2), o);
        signal_counter(p, CTL_WOC + seg * 16);
    }
}

DEV void worker_mlp1(const Ptrs& p, int grp, int j, int wave, int lane, char* smem) {
    __amdgpu_buffer_rsrc_t rs = mkrs(p.ws);
    v4u B[16][2];
    const int nt0 = j * 8 + wave * 2;
#pragma unroll
    for (int ks = 0; ks < 16; ++ks)
#pragma unroll
        for (int n = 0; n < 2; ++n) B[ks][n] = bld4<0>(rs, (unsigned)(WS_W1 + (size_t)ks * 112 * 1024 + (nt0 + n) * 1024 + lane * 16));
    const int g = lane >> 4, l15 = lane & 15;
    v2u bv[2];
#pragma unroll
    for (int n = 0; n < 2; ++n) bv[n] = *(const v2u*)(p.b1 + (nt0 + n) * 16 + 4 * g);
    const float* mod = (const float*)(p.ws + WS_MOD);
    float sb[8], cb1[8];
#pragma unroll
    for (int i = 0; i < 8; ++i) { sb[i] = mod[1536 + lane * 8 + i]; cb1[i] = 1.f + mod[2048 + lane * 8 + i]; }
    for (int seg = grp; seg < NSEG; seg += 4) {
        wait_counter(p, CTL_WOC + seg * 16, 16, wave, seg >= NSEG - 4);
        v4u xr[8];
#pragma unroll
        for (int i = 0; i < 8; ++i) xr[i] = bld4<16>(rs, (unsigned)(WS_X1 + (size_t)(seg * 32 + wave * 8 + ((i + j) & 7)) * 1024 + lane * 16));
#pragma unroll
        for (int i = 0; i < 8; ++i) {
            float v[8];
            unpack8(xr[i], v);
            ln_row(v);
#pragma unroll
            for (int q = 0; q < 8; ++q) v[q] = fmaf(v[q], cb1[q], sb[q]);
            ln_row(v);
            const int r = wave * 8 + ((i + j) & 7);
            *(v4u*)(smem + ((r >> 4) * 16 + (lane >> 2)) * 1024 + ((r & 15) + 16 * (lane & 3)) * 16) = pack8(v);
        }
        BAR_ONLY();
        v4f acc[2][2] = {{{0, 0, 0, 0}, {0, 0, 0, 0}}, {{0, 0, 0, 0}, {0, 0, 0, 0}}};
#pragma unroll
        for (int ks = 0; ks < 16; ++ks) {
            v4u a0 = *(const v4u*)(smem + ks * 1024 + lane * 16), a1 = *(const v4u*)(smem + 16384 + ks * 1024 + lane * 16);
#pragma unroll
            for (int n = 0; n < 2; ++n) { acc[0][n] = mfma16(B[ks][n], a0, acc[0][n]); acc[1][n] = mfma16(B[ks][n], a1, acc[1][n]); }
        }
#pragma unroll
        for (int n = 0; n < 2; ++n) {
            const int nt = nt0 + n;
            const float b0 = lo16f(bv[n].x), b1 = hi16f(bv[n].x), b2 = lo16f(bv[n].y), b3 = hi16f(bv[n].y);
#pragma unroll
            for (int m = 0; m < 2; ++m) {
                float r0 = gelu_tanh(acc[m][n][0] + b0), r1 = gelu_tanh(acc[m][n][1] + b1);
                float r2 = gelu_tanh(acc[m][n][2] + b2), r3 = gelu_tanh(acc[m][n][3] + b3);
                v2u o; o.x = pack2(r0, r1); o.y = pack2(r2, r3);
                unsigned off = (unsigned)(WS_G + ((size_t)(seg * 2 + m) * 56 + (nt >> 1)) * 1024 + (l15 + 16 * (2 * (nt & 1) + (g >> 1))) * 16 + (g & 1) * 8);
                bst2<16>(rs, off, o);
            }
        }
        signal_counter(p, CTL_M1C + seg * 16);
    }
}

DEV void worker_mlp2(const Ptrs& p, int grp, int j, int wave, int lane, char* smem) {
    __amdgpu_buffer_rsrc_t rs = mkrs(p.ws);
    v4u B[14][2];
#pragma unroll
    for (int ks = 0; ks < 14; ++ks)
#pragma unroll
        for (int n = 0; n < 2; ++n) B[ks][n] = bld4<0>(rs, (unsigned)(WS_W2 + (size_t)(wave * 14 + (ks + j) % 14) * 32768 + (2 * j + n) * 1024 + lane * 16));
    const int g = lane >> 4, l15 = lane & 15;
    const int c = (2 * j + (wave & 1)) * 16 + 4 * g;
    const v4f gb = *(const v4f*)((const float*)(p.ws + WS_MOD) + 2560 + c);
    const v2u bv = *(const v2u*)(p.b2 + c);
    const float b0 = lo16f(bv.x), b1 = hi16f(bv.x), b2 = lo16f(bv.y), b3 = hi16f(bv.y);
    for (int seg = grp; seg < NSEG; seg += 4) {
        const int row = (seg * 2 + (wave >> 1)) * 16 + l15;
        wait_counter(p, CTL_M1C + seg * 16, 14, wave, seg >= NSEG - 4);
        const v2u xv = bld2<16>(rs, (unsigned)(WS_X1 + ((size_t)row * 512 + c) * 2));
        v4f acc[2][2] = {{{0, 0, 0, 0}, {0, 0, 0, 0}}, {{0, 0, 0, 0}, {0, 0, 0, 0}}};
        const unsigned aoff = (unsigned)(WS_G + (size_t)seg * 2 * 57344 + wave * 14 * 1024 + lane * 16);
        v4u A[14][2];
#pragma unroll
        for (int k = 0; k < 14; ++k) { const unsigned ko = (unsigned)((k + j) % 14) * 1024; A[k][0] = bld4<16>(rs, aoff + ko); A[k][1] = bld4<16>(rs, aoff + 57344 + ko); }
#pragma unroll
        for (int k = 0; k < 14; ++k)
#pragma unroll
            for (int m = 0; m < 2; ++m)
#pragma unroll
                for (int n = 0; n < 2; ++n) acc[m][n] = mfma16(B[k][n], A[k][m], acc[m][n]);
        v4f r = ksplit_reduce(acc, smem, wave, lane);
        float r0 = fmaf(gb[0], r[0] + b0, lo16f(xv.x)), r1 = fmaf(gb[1], r[1] + b1, hi16f(xv.x));
        float r2 = fmaf(gb[2], r[2] + b2, lo16f(xv.y)), r3 = fmaf(gb[3], r[3] + b3, hi16f(xv.y));
        v2u o; o.x = pack2(r0, r1); o.y = pack2(r2, r3);
        *(v2u*)(p.out + (size_t)row * 512 + c) = o;
        BAR_ONLY();
    }
}

__global__ void __launch_bounds__(256, 1) k_mega(Ptrs p) {
    extern __shared__ __attribute__((aligned(16))) char smem[];
    const int b = blockIdx.x;
#define RSTAMP(i)
    RSTAMP(0);
    if (b < NHEAD) { head_main<true>(p, b, smem); RSTAMP(3); return; }
    const int wave = __builtin_amdgcn_readfirstlane(threadIdx.x >> 6), lane = threadIdx.x & 63;
    const int w = b - NHEAD;
    for (int t = w; t < 512; t += NWORK) {
        task_qkv<16>(p, t >> 5, t & 31, wave, lane);
        DRAIN();
        BAR_ONLY();
        if (threadIdx.x == 0) st_ctl(p, CTL_QKVF + t, 1u);
        if (t == w) RSTAMP(2);
    }
    RSTAMP(1);
    const int x = w & 7, idx = w >> 3, grp = x >> 1;
    if ((x & 1) == 0) {
        if (idx < 8) worker_att(p, grp, idx, wave, lane);
        else if (idx < 24) worker_wo(p, grp, idx - 8, wave, lane, smem);
        else if (idx < 26) worker_mlp2(p, grp, idx - 24 + 14, wave, lane, smem);
    } else {
        if (idx < 14) worker_mlp1(p, grp, idx, wave, lane, smem);
        else worker_mlp2(p, grp, idx - 14, wave, lane, smem);
    }
    RSTAMP(3);
}

extern "C" void kernel_launch(void* const* d_in, const int* in_sizes, int n_in, void* d_out, int out_size, void* d_ws, size_t ws_size, hipStream_t stream) {
    Ptrs p;
    p.x = (const bf16_t*)d_in[0]; p.mem0 = (const bf16_t*)d_in[1]; p.wq = (const bf16_t*)d_in[2]; p.wk = (const bf16_t*)d_in[3];
    p.wv = (const bf16_t*)d_in[4]; p.wo = (const bf16_t*)d_in[5]; p.w1 = (const bf16_t*)d_in[8]; p.b1 = (const bf16_t*)d_in[9];
    p.w2 = (const bf16_t*)d_in[10]; p.b2 = (const bf16_t*)d_in[11]; p.c = (const bf16_t*)d_in[12]; p.wmod = (const bf16_t*)d_in[13];
    p.ws = (char*)d_ws; p.out = (bf16_t*)d_out;
    (void)hipFuncSetAttribute((const void*)k_mega, hipFuncAttributeMaxDynamicSharedMemorySize, HL_END);
    k_prep<<<PREP_BLOCKS, 256, 0, stream>>>(p);
    k_ln1<<<136, 256, 0, stream>>>(p);
    k_mega<<<256, 256, HL_END, stream>>>(p);
}
```

```cpp
constexpr int CTL_QKVF = 1024;
constexpr int CTL_KVM  = 2048;
constexpr int CTL_ATTC = 4096;
constexpr int CTL_WOC  = CTL_ATTC + 1024;
constexpr int CTL_M1C  = CTL_WOC + 1024;
constexpr int NWORK = 256 - NHB;

#define WST(i)
DEV unsigned ld_ctl(const Ptrs& p, int idx) { return __hip_atomic_load((GAS unsigned*)(p.ws + WS_CTL) + idx, RLX_AGENT); }
DEV void st_ctl(const Ptrs& p, int idx, unsigned v) { __hip_atomic_store((GAS unsigned*)(p.ws + WS_CTL) + idx, v, RLX_AGENT); }
DEV void add_ctl(const Ptrs& p, int idx) { __hip_atomic_fetch_add((GAS unsigned*)(p.ws + WS_CTL) + idx, 1u, RLX_AGENT); }
#define BAR_ONLY() asm volatile("s_waitcnt lgkmcnt(0)\n\ts_barrier" ::: "memory")
#define DRAIN() asm volatile("s_waitcnt vmcnt(0)" ::: "memory")
DEV void nap(bool fast) { if (fast) __builtin_amdgcn_s_sleep(3); else __builtin_amdgcn_s_sleep(40); }
DEV void wait_counter(const Ptrs& p, int idx, unsigned want, int wave, bool fast) {
    if (wave == 0) {
        unsigned spins = 0;
        while ((unsigned)__builtin_amdgcn_readfirstlane(ld_ctl(p, idx)) < want && ++spins < SPIN_CAP) nap(fast);
    }
    BAR_ONLY();
}
DEV void signal_counter(const Ptrs& p, int idx) {
    DRAIN();
    BAR_ONLY();
    if (threadIdx.x == 0) add_ctl(p, idx);
}

DEV void att_task(const Ptrs& p, int seg, int slot, int wave, int lane) {
    const int h = slot * 2 + (wave >> 1);
    unsigned spins = 0;
    while ((__builtin_amdgcn_readfirstlane(ld_ctl(p, CTL_QKVF + (seg >> 1) * 32 + h)) == 0 || __builtin_amdgcn_readfirstlane(ld_ctl(p, CTL_KVM + seg * 32 + h)) == 0) && ++spins < SPIN_CAP)
        nap(seg >= NSEG - 4);
    asm volatile("" ::: "memory");
    WST(4);
    att_half<16, 16>(p, seg, h, wave & 1, lane);
    WST(5);
    signal_counter(p, CTL_ATTC + seg * 16);
}

DEV v4f ksplit_reduce(v4f (&acc)[2][2], char* smem, int wave, int lane) {
#pragma unroll
    for (int m = 0; m < 2; ++m)
#pragma unroll
        for (int n = 0; n < 2; ++n) *(v4f*)(smem + ((wave * 4 + m * 2 + n) * 64 + lane) * 16) = acc[m][n];
    BAR_ONLY();
    v4f r = *(const v4f*)(smem + ((0 * 4 + wave) * 64 + lane) * 16);
#pragma unroll
    for (int w = 1; w < 4; ++w) r += *(const v4f*)(smem + ((w * 4 + wave) * 64 + lane) * 16);
    return r;
}

DEV void worker_wo(const Ptrs& p, int grp, int j, int wave, int lane, char* smem) {
    __amdgpu_buffer_rsrc_t rs = mkrs(p.ws);
    v4u B[32][2];
#pragma unroll
    for (int ks = 0; ks < 32; ++ks)
#pragma unroll
        for (int n = 0; n < 2; ++n) B[ks][n] = bld4<0>(rs, (unsigned)(WS_WO + (size_t)(wave * 32 + ((ks + 2 * j) & 31)) * 32768 + (2 * j + n) * 1024 + lane * 16));
    const int g = lane >> 4, l15 = lane & 15;
    const int c = (2 * j + (wave & 1)) * 16 + 4 * g;
    const v4f ga = *(const v4f*)((const float*)(p.ws + WS_MOD) + 1024 + c);
    for (int seg = grp; seg < NSEG; seg += 4) {
        const int row = (seg * 2 + (wave >> 1)) * 16 + l15;
        const v2u xv = *(const v2u*)(p.x + (size_t)row * 512 + c);
#pragma unroll
        for (int ks = 0; ks < 30; ++ks) asm volatile("" : "+a"(B[ks][0]), "+a"(B[ks][1]));
        att_task(p, seg, j, wave, lane);
        wait_counter(p, CTL_ATTC + seg * 16, 16, wave, true);
        WST(6);
        v4f acc[2][2] = {{{0, 0, 0, 0}, {0, 0, 0, 0}}, {{0, 0, 0, 0}, {0, 0, 0, 0}}};
        const unsigned aoff = (unsigned)(WS_O + (size_t)seg * 2 * 131072 + wave * 32768 + lane * 16);
        v4u A[3][8][2];
#pragma unroll
        for (int bt = 0; bt < 2; ++bt)
#pragma unroll
            for (int k = 0; k < 8; ++k) { const unsigned ko = (unsigned)((bt * 8 + k + 2 * j) & 31) * 1024; A[bt][k][0] = bld4<16>(rs, aoff + ko); A[bt][k][1] = bld4<16>(rs, aoff + 131072 + ko); }
#pragma unroll
        for (int bt = 0; bt < 4; ++bt) {
            if (bt + 2 < 4) {
#pragma unroll
                for (int k = 0; k < 8; ++k) { const unsigned ko = (unsigned)(((bt + 2) * 8 + k + 2 * j) & 31) * 1024; A[(bt + 2) % 3][k][0] = bld4<16>(rs, aoff + ko); A[(bt + 2) % 3][k][1] = bld4<16>(rs, aoff + 131072 + ko); }
            }
            __builtin_amdgcn_sched_barrier(0);
#pragma unroll
            for (int k = 0; k < 8; ++k)
#pragma unroll
                for (int m = 0; m < 2; ++m)
#pragma unroll
                    for (int n = 0; n < 2; ++n) acc[m][n] = mfma16(B[bt * 8 + k][n], A[bt % 3][k][m], acc[m][n]);
            asm volatile("" : "+v"(acc[0][0]), "+v"(acc[0][1]), "+v"(acc[1][0]), "+v"(acc[1][1]));
            __builtin_amdgcn_sched_barrier(0);
        }
        WST(7);
        v4f r = ksplit_reduce(acc, smem, wave, lane);
        float r0 = fmaf(ga[0], r[0], lo16f(xv.x)), r1 = fmaf(ga[1], r[1], hi16f(xv.x));
        float r2 = fmaf(ga[2], r[2], lo16f(xv.y)), r3 = fmaf(ga[3], r[3], hi16f(xv.y));
        v2u o; o.x = pack2(r0, r1); o.y = pack2(r2, r3);
        bst2<16>(rs, (unsigned)(WS_X1 + ((size_t)row * 512 + c) * 2), o);
        signal_counter(p, CTL_WOC + seg * 16);
    }
}

DEV void worker_mlp1(const Ptrs& p, int grp, int j, int wave, int lane, char* smem) {
    __amdgpu_buffer_rsrc_t rs = mkrs(p.ws);
    v4u B[16][2];
    const int nt0 = j * 8 + wave * 2;
#pragma unroll
    for (int ks = 0; ks < 16; ++ks)
#pragma unroll
        for (int n = 0; n < 2; ++n) B[ks][n] = bld4<0>(rs, (unsigned)(WS_W1 + (size_t)ks * 112 * 1024 + (nt0 + n) * 1024 + lane * 16));
    const int g = lane >> 4, l15 = lane & 15;
    v2u bv[2];
#pragma unroll
    for (int n = 0; n < 2; ++n) bv[n] = *(const v2u*)(p.b1 + (nt0 + n) * 16 + 4 * g);
    const float* mod = (const float*)(p.ws + WS_MOD);
    float sb[8], cb1[8];
#pragma unroll
    for (int i = 0; i < 8; ++i) { sb[i] = mod[1536 + lane * 8 + i]; cb1[i] = 1.f + mod[2048 + lane * 8 + i]; }
    for (int seg = grp; seg < NSEG; seg += 4) {
        wait_counter(p, CTL_WOC + seg * 16, 16, wave, seg >= NSEG - 4);
        WST(4);
        v4u xr[8];
#pragma unroll
        for (int i = 0; i < 8; ++i) xr[i] = bld4<16>(rs, (unsigned)(WS_X1 + (size_t)(seg * 32 + wave * 8 + ((i + j) & 7)) * 1024 + lane * 16));
#pragma unroll
        for (int i = 0; i < 8; ++i) {
            float v[8];
            unpack8(xr[i], v);
            ln_row(v);
#pragma unroll
            for (int q = 0; q < 8; ++q) v[q] = fmaf(v[q], cb1[q], sb[q]);
            ln_row(v);
            const int r = wave * 8 + ((i + j) & 7);
            *(v4u*)(smem + ((r >> 4) * 16 + (lane >> 2)) * 1024 + ((r & 15) + 16 * (lane & 3)) * 16) = pack8(v);
        }
        BAR_ONLY();
        WST(5);
        v4f acc[2][2] = {{{0, 0, 0, 0}, {0, 0, 0, 0}}, {{0, 0, 0, 0}, {0, 0, 0, 0}}};
#pragma unroll
        for (int ks = 0; ks < 16; ++ks) {
            v4u a0 = *(const v4u*)(smem + ks * 1024 + lane * 16), a1 = *(const v4u*)(smem + 16384 + ks * 1024 + lane * 16);
#pragma unroll
            for (int n = 0; n < 2; ++n) { acc[0][n] = mfma16(B[ks][n], a0, acc[0][n]); acc[1][n] = mfma16(B[ks][n], a1, acc[1][n]); }
        }
#pragma unroll
        for (int m = 0; m < 2; ++m) {
            unsigned pk[2][2];
#pragma unroll
            for (int n = 0; n < 2; ++n) {
                const float b0 = lo16f(bv[n].x), b1 = hi16f(bv[n].x), b2 = lo16f(bv[n].y), b3 = hi16f(bv[n].y);
                pk[n][0] = pack2(gelu_tanh(acc[m][n][0] + b0), gelu_tanh(acc[m][n][1] + b1));
                pk[n][1] = pack2(gelu_tanh(acc[m][n][2] + b2), gelu_tanh(acc[m][n][3] + b3));
            }
            auto t0 = __builtin_amdgcn_permlane16_swap(pk[0][0], pk[1][0], false, false);
            auto t1 = __builtin_amdgcn_permlane16_swap(pk[0][1], pk[1][1], false, false);
            v4u ov = {t0[0], t1[0], t0[1], t1[1]};
            bst4<16>(rs, (unsigned)(WS_G + ((size_t)(seg * 2 + m) * 56 + (nt0 >> 1)) * 1024 + (l15 + 16 * (2 * (g & 1) + (g >> 1))) * 16), ov);
        }
        WST(6);
        signal_counter(p, CTL_M1C + seg * 16);
    }
}

DEV void worker_mlp2(const Ptrs& p, int grp, int j, int wave, int lane, char* smem) {
    __amdgpu_buffer_rsrc_t rs = mkrs(p.ws);
    v4u B[14][2];
#pragma unroll
    for (int ks = 0; ks < 14; ++ks)
#pragma unroll
        for (int n = 0; n < 2; ++n) B[ks][n] = bld4<0>(rs, (unsigned)(WS_W2 + (size_t)(wave * 14 + (ks + j) % 14) * 32768 + (2 * j + n) * 1024 + lane * 16));
    const int g = lane >> 4, l15 = lane & 15;
    const int c = (2 * j + (wave & 1)) * 16 + 4 * g;
    const v4f gb = *(const v4f*)((const float*)(p.ws + WS_MOD) + 2560 + c);
    const v2u bv = *(const v2u*)(p.b2 + c);
    const float b0 = lo16f(bv.x), b1 = hi16f(bv.x), b2 = lo16f(bv.y), b3 = hi16f(bv.y);
    for (int seg = grp; seg < NSEG; seg += 4) {
        const int row = (seg * 2 + (wave >> 1)) * 16 + l15;
        wait_counter(p, CTL_M1C + seg * 16, 14, wave, seg >= NSEG - 4);
        WST(4);
        const v2u xv = bld2<16>(rs, (unsigned)(WS_X1 + ((size_t)row * 512 + c) * 2));
        v4f acc[2][2] = {{{0, 0, 0, 0}, {0, 0, 0, 0}}, {{0, 0, 0, 0}, {0, 0, 0, 0}}};
        const unsigned aoff = (unsigned)(WS_G + (size_t)seg * 2 * 57344 + wave * 14 * 1024 + lane * 16);
        v4u A[14][2];
#pragma unroll
        for (int k = 0; k < 14; ++k) { const unsigned ko = (unsigned)((k + j) % 14) * 1024; A[k][0] = bld4<16>(rs, aoff + ko); A[k][1] = bld4<16>(rs, aoff + 57344 + ko); }
#pragma unroll
        for (int k = 0; k < 14; ++k)
#pragma unroll
            for (int m = 0; m < 2; ++m)
#pragma unroll
                for (int n = 0; n < 2; ++n) acc[m][n] = mfma16(B[k][n], A[k][m], acc[m][n]);
        WST(5);
        v4f r = ksplit_reduce(acc, smem, wave, lane);
        float r0 = fmaf(gb[0], r[0] + b0, lo16f(xv.x)), r1 = fmaf(gb[1], r[1] + b1, hi16f(xv.x));
        float r2 = fmaf(gb[2], r[2] + b2, lo16f(xv.y)), r3 = fmaf(gb[3], r[3] + b3, hi16f(xv.y));
        v2u o; o.x = pack2(r0, r1); o.y = pack2(r2, r3);
        *(v2u*)(p.out + (size_t)row * 512 + c) = o;
        BAR_ONLY();
    }
}

__global__ void __launch_bounds__(256, 1) k_mega(Ptrs p) {
    extern __shared__ __attribute__((aligned(16))) char smem[];
    const int b = blockIdx.x;
#define RSTAMP(i)
    RSTAMP(0);
    if (b < NHB) { head_main(p, b, smem); RSTAMP(3); return; }
    const int wave = __builtin_amdgcn_readfirstlane(threadIdx.x >> 6), lane = threadIdx.x & 63;
    const int w = b - NHB;
    for (int t = w; t < 1024; t += NWORK) {
        task_qkv<16>(p, t >> 5, t & 31, wave, lane);
        DRAIN();
        BAR_ONLY();
        if (threadIdx.x == 0) st_ctl(p, CTL_QKVF + t, 1u);
        if (t == w) RSTAMP(2);
    }
    RSTAMP(1);
    const int x = w & 7, idx = w >> 3, grp = x >> 1;
    if ((x & 1) == 0) {
        if (idx < 16) worker_wo(p, grp, idx, wave, lane, smem);
        else worker_mlp2(p, grp, idx - 16, wave, lane, smem);
    } else {
        if (idx < 14) worker_mlp1(p, grp, idx, wave, lane, smem);
        else if (idx < 22) worker_mlp2(p, grp, idx - 14 + 8, wave, lane, smem);
    }
    RSTAMP(3);
}

extern "C" void kernel_launch(void* const* d_in, const int* in_sizes, int n_in, void* d_out, int out_size, void* d_ws, size_t ws_size, hipStream_t stream) {
    Ptrs p;
    p.x = (const bf16_t*)d_in[0]; p.mem0 = (const bf16_t*)d_in[1]; p.wq = (const bf16_t*)d_in[2]; p.wk = (const bf16_t*)d_in[3];
    p.wv = (const bf16_t*)d_in[4]; p.wo = (const bf16_t*)d_in[5]; p.w1 = (const bf16_t*)d_in[8]; p.b1 = (const bf16_t*)d_in[9];
    p.w2 = (const bf16_t*)d_in[10]; p.b2 = (const bf16_t*)d_in[11]; p.c = (const bf16_t*)d_in[12]; p.wmod = (const bf16_t*)d_in[13];
    p.ws = (char*)d_ws; p.out = (bf16_t*)d_out;
    (void)hipFuncSetAttribute((const void*)k_mega, hipFuncAttributeMaxDynamicSharedMemorySize, HL_END);
    k_prep<<<PREP_BLOCKS, 256, 0, stream>>>(p);
    k_ln1<<<136, 256, 0, stream>>>(p);
    k_mega<<<256, 256, HL_END, stream>>>(p);
}
```

```cpp
#include <hip/hip_runtime.h>
#include <hip/hip_bf16.h>
#include <stdint.h>

typedef __bf16 bf16_t;
typedef __bf16 v2bf __attribute__((ext_vector_type(2)));
typedef __bf16 v4bf __attribute__((ext_vector_type(4)));
typedef __bf16 v8bf __attribute__((ext_vector_type(8)));
typedef __bf16 v16bf __attribute__((ext_vector_type(16)));
typedef float v4f __attribute__((ext_vector_type(4)));
typedef unsigned v4u __attribute__((ext_vector_type(4)));
typedef unsigned v2u __attribute__((ext_vector_type(2)));
typedef unsigned v8u __attribute__((ext_vector_type(8)));
typedef short v4s __attribute__((ext_vector_type(4)));
#define GAS __attribute__((address_space(1)))
#define LAS __attribute__((address_space(3)))
#define RLX_AGENT __ATOMIC_RELAXED, __HIP_MEMORY_SCOPE_AGENT
#define DEV __device__ __forceinline__

constexpr int S_TOK = 2048, DM = 512, NHEAD = 32, DH = 128, MEMN = 8, SEG = 32, NSEG = 64, FF = 1792;

constexpr size_t WS_CTL   = 0;
constexpr size_t WS_MOD   = 65536;
constexpr size_t WS_FVP   = WS_MOD + 16384;
constexpr size_t WS_CNP   = WS_FVP + 4096;
constexpr size_t WS_ACC   = WS_CNP + 262144;
constexpr size_t WS_H     = WS_ACC + 2097152;
constexpr size_t WS_WQKV  = WS_H + 2097152;
constexpr size_t WS_WO    = WS_WQKV + 12582912;
constexpr size_t WS_W1    = WS_WO + 4194304;
constexpr size_t WS_W2    = WS_W1 + 1835008;
constexpr size_t WS_Q     = WS_W2 + 1835008;
constexpr size_t WS_K     = WS_Q + 16777216;
constexpr size_t WS_V     = WS_K + 16777216;
constexpr size_t WS_KM    = WS_V + 16777216;
constexpr size_t WS_VM    = WS_KM + 8388608;
constexpr size_t WS_O     = WS_VM + 4194304;
constexpr size_t WS_X1    = WS_O + 16777216;
constexpr size_t WS_G     = WS_X1 + 2097152;
constexpr size_t WS_END   = WS_G + 7340032;
constexpr int CTL_HMAX = 0;
constexpr int CTL_CM2  = 128;

DEV float bf2f(bf16_t x) { return (float)x; }
DEV float rbf(float x) { return (float)(bf16_t)x; }
DEV unsigned pack2(float a, float b) { v2bf v; v[0] = (bf16_t)a; v[1] = (bf16_t)b; return __builtin_bit_cast(unsigned, v); }
DEV float lo16f(unsigned w) { return __uint_as_float(w << 16); }
DEV float hi16f(unsigned w) { return __uint_as_float(w & 0xffff0000u); }
DEV v4f mfma16(v4u a, v4u b, v4f c) { return __builtin_amdgcn_mfma_f32_16x16x32_bf16(__builtin_bit_cast(v8bf, a), __builtin_bit_cast(v8bf, b), c, 0, 0, 0); }
DEV v4f smfmac(v4u a, v8u b, v4f c, int idx) { return __builtin_amdgcn_smfmac_f32_16x16x64_bf16(__builtin_bit_cast(v8bf, a), __builtin_bit_cast(v16bf, b), c, idx, 0, 0); }
DEV __amdgpu_buffer_rsrc_t mkrs(const void* p) { return __builtin_amdgcn_make_buffer_rsrc((void*)p, 0, 0x7fffffff, 0x00020000); }
template <int AUX> DEV v4u bld4(__amdgpu_buffer_rsrc_t r, unsigned off) { return __builtin_amdgcn_raw_buffer_load_b128(r, off, 0, AUX); }
template <int AUX> DEV v2u bld2(__amdgpu_buffer_rsrc_t r, unsigned off) { return __builtin_amdgcn_raw_buffer_load_b64(r, off, 0, AUX); }
template <int AUX> DEV void bst4(__amdgpu_buffer_rsrc_t r, unsigned off, v4u v) { __builtin_amdgcn_raw_buffer_store_b128(v, r, off, 0, AUX); }
template <int AUX> DEV void bst2(__amdgpu_buffer_rsrc_t r, unsigned off, v2u v) { __builtin_amdgcn_raw_buffer_store_b64(v, r, off, 0, AUX); }
DEV float xmax16_32(float x) {
    auto a = __builtin_amdgcn_permlane16_swap(__float_as_uint(x), __float_as_uint(x), false, false);
    x = fmaxf(__uint_as_float(a[0]), __uint_as_float(a[1]));
    auto b = __builtin_amdgcn_permlane32_swap(__float_as_uint(x), __float_as_uint(x), false, false);
    return fmaxf(__uint_as_float(b[0]), __uint_as_float(b[1]));
}
DEV float xsum16_32(float x) {
    auto a = __builtin_amdgcn_permlane16_swap(__float_as_uint(x), __float_as_uint(x), false, false);
    x = __uint_as_float(a[0]) + __uint_as_float(a[1]);
    auto b = __builtin_amdgcn_permlane32_swap(__float_as_uint(x), __float_as_uint(x), false, false);
    return __uint_as_float(b[0]) + __uint_as_float(b[1]);
}
template <int CTRL> DEV float dpp_f(float x) { return __uint_as_float((unsigned)__builtin_amdgcn_update_dpp(0, (int)__float_as_uint(x), CTRL, 0xf, 0xf, false)); }
DEV float wave_sum(float x) {
    x += dpp_f<0x128>(x); x += dpp_f<0x124>(x); x += dpp_f<0x122>(x); x += dpp_f<0x121>(x);
    return xsum16_32(x);
}
DEV unsigned wave_umax(unsigned x) {
#pragma unroll
    for (int d = 1; d < 64; d <<= 1) { unsigned o = __shfl_xor(x, d); x = o > x ? o : x; }
    return x;
}
DEV float wave_max(float x) {
#pragma unroll
    for (int d = 1; d < 64; d <<= 1) x = fmaxf(x, __shfl_xor(x, d));
    return x;
}
DEV float fold2(float a, float b) {
    auto t = __builtin_amdgcn_permlane32_swap(__float_as_uint(a), __float_as_uint(b), false, false);
    return __uint_as_float(t[0]) + __uint_as_float(t[1]);
}
struct Ptrs {
    const bf16_t *x, *mem0, *wq, *wk, *wv, *wo, *w1, *b1, *w2, *b2, *c, *wmod;
    char* ws; bf16_t* out;
};
constexpr int PREP_MOD_BLOCKS = 48;
constexpr int PREP_ZERO_BLOCKS = 132;
constexpr int PREP_PACK_TILES = 1536 + 512 + 224 + 224;
constexpr int PREP_BLOCKS = PREP_MOD_BLOCKS + PREP_ZERO_BLOCKS + PREP_PACK_TILES / 2;

DEV void unpack8f(v4u w, float (&v)[8]) {
    v[0] = lo16f(w.x); v[1] = hi16f(w.x); v[2] = lo16f(w.y); v[3] = hi16f(w.y);
    v[4] = lo16f(w.z); v[5] = hi16f(w.z); v[6] = lo16f(w.w); v[7] = hi16f(w.w);
}
constexpr int PK_LDW = 136;
struct PackDesc { const bf16_t* src; int ld; char* dst; int kind; int idx; };
DEV PackDesc pack_desc(const Ptrs& p, int b) {
    PackDesc d; d.kind = 0; d.idx = 0;
    if (b < 1536) {
        int pidx = b / 512, r = b % 512, ks = r >> 5, h = r & 31;
        const bf16_t* w = pidx == 0 ? p.wq : (pidx == 1 ? p.wk : p.wv);
        d.src = w + (size_t)ks * 32 * 4096 + h * 128; d.ld = 4096; d.dst = p.ws + WS_WQKV + ((size_t)(h * 3 + pidx) * 16 + ks) * 8192;
        if (pidx == 2) { d.kind = 1; d.idx = r; }
    } else if (b < 2048) {
        b -= 1536; int ks = b >> 2, cb = b & 3;
        d.src = p.wo + (size_t)ks * 32 * 512 + cb * 128; d.ld = 512; d.dst = p.ws + WS_WO + ((size_t)ks * 32 + cb * 8) * 1024; d.kind = 2; d.idx = ks * 512 + cb * 128;
    } else if (b < 2272) {
        b -= 2048; int ks = b / 14, cb = b % 14;
        d.src = p.w1 + (size_t)ks * 32 * FF + cb * 128; d.ld = FF; d.dst = p.ws + WS_W1 + ((size_t)ks * 112 + cb * 8) * 1024;
    } else {
        b -= 2272; int ks = b >> 2, cb = b & 3;
        d.src = p.w2 + (size_t)ks * 32 * 512 + cb * 128; d.ld = 512; d.dst = p.ws + WS_W2 + ((size_t)ks * 32 + cb * 8) * 1024;
    }
    return d;
}
DEV void pack_emit(const Ptrs& p, const PackDesc& d, const unsigned short* lds, float* red) {
    const int tid = threadIdx.x;
    float ss = 0.f;
#pragma unroll
    for (int i = 0; i < 2; ++i) {
        int item = tid + 256 * i;
        int nt = item >> 6, lane = item & 63, n = lane & 15, g = lane >> 4;
        typedef short v4i16_t __attribute__((ext_vector_type(4)));
        const unsigned short* ta = lds + (8 * g + (n >> 2)) * PK_LDW + nt * 16 + (n & 3) * 4;
        const v2u t0 = __builtin_bit_cast(v2u, __builtin_amdgcn_ds_read_tr16_b64_v4i16((LAS v4i16_t*)ta));
        const v2u t1 = __builtin_bit_cast(v2u, __builtin_amdgcn_ds_read_tr16_b64_v4i16((LAS v4i16_t*)(ta + 4 * PK_LDW)));
        const v4u o = {t0.x, t0.y, t1.x, t1.y};
        *(v4u*)(d.dst + nt * 1024 + lane * 16) = o;
        if (d.kind) {
            float f[8];
            unpack8f(o, f);
            float s = 0.f;
#pragma unroll
            for (int j = 0; j < 8; ++j) s = fmaf(f[j], f[j], s);
            ss += s;
            if (d.kind == 2) { s = xsum16_32(s); if (g == 0) ((float*)(p.ws + WS_CNP))[d.idx + nt * 16 + n] = s; }
        }
    }
    if (d.kind == 1) {
        ss = wave_sum(ss);
        if ((tid & 63) == 0) red[tid >> 6] = ss;
        __syncthreads();
        if (tid == 0) ((float*)(p.ws + WS_FVP))[d.idx] = (red[0] + red[1]) + (red[2] + red[3]);
    }
}

__global__ void __launch_bounds__(256) k_prep(Ptrs p) {
    __shared__ __attribute__((aligned(16))) unsigned short lds[2 * 32 * 136];
    __shared__ float red[32 * 64];
    const int tid = threadIdx.x;
    int b = blockIdx.x;
    if (b < PREP_MOD_BLOCKS) {
        const int cg = tid & 7, kg = tid >> 3, col0 = b * 64 + cg * 8;
        float acc[8] = {0, 0, 0, 0, 0, 0, 0, 0};
        v4u wv[16];
#pragma unroll
        for (int i = 0; i < 16; ++i) wv[i] = *(const v4u*)(p.wmod + (size_t)(kg * 16 + i) * 3072 + col0);
        const v4u c0 = *(const v4u*)(p.c + kg * 16), c1 = *(const v4u*)(p.c + kg * 16 + 8);
        float cv[16];
        { float t[8]; unpack8f(c0, t); for (int i = 0; i < 8; ++i) cv[i] = t[i]; unpack8f(c1, t); for (int i = 0; i < 8; ++i) cv[8 + i] = t[i]; }
#pragma unroll
        for (int i = 0; i < 16; ++i) {
            const float sc = rbf(cv[i] / (1.f + __expf(-cv[i])));
            const v4u w = wv[i];
            acc[0] += sc * lo16f(w.x); acc[1] += sc * hi16f(w.x); acc[2] += sc * lo16f(w.y); acc[3] += sc * hi16f(w.y);
            acc[4] += sc * lo16f(w.z); acc[5] += sc * hi16f(w.z); acc[6] += sc * lo16f(w.w); acc[7] += sc * hi16f(w.w);
        }
#pragma unroll
        for (int j = 0; j < 8; ++j) red[kg * 64 + cg * 8 + j] = acc[j];
        __syncthreads();
        if (tid < 64) {
            float s = 0.f;
            for (int i = 0; i < 32; ++i) s += red[i * 64 + tid];
            ((float*)(p.ws + WS_MOD))[b * 64 + tid] = rbf(s);
        }
        return;
    }
    b -= PREP_MOD_BLOCKS;
    if (b < PREP_ZERO_BLOCKS) {
        char* base = (b < 4) ? (p.ws + WS_CTL + (size_t)b * 16384) : (p.ws + WS_ACC + (size_t)(b - 4) * 16384);
        v4u z = {0, 0, 0, 0};
#pragma unroll
        for (int i = 0; i < 4; ++i) *(v4u*)(base + (i * 256 + tid) * 16) = z;
        return;
    }
    b -= PREP_ZERO_BLOCKS;
    const PackDesc d0 = pack_desc(p, 2 * b), d1 = pack_desc(p, 2 * b + 1);
    const int row = tid >> 4, ch = tid & 15;
    v4u v0 = *(const v4u*)(d0.src + (size_t)row * d0.ld + ch * 8), v1 = *(const v4u*)(d0.src + (size_t)(row + 16) * d0.ld + ch * 8);
    v4u v2 = *(const v4u*)(d1.src + (size_t)row * d1.ld + ch * 8), v3 = *(const v4u*)(d1.src + (size_t)(row + 16) * d1.ld + ch * 8);
    *(v4u*)(lds + row * PK_LDW + ch * 8) = v0; *(v4u*)(lds + (row + 16) * PK_LDW + ch * 8) = v1;
    *(v4u*)(lds + (32 + row) * PK_LDW + ch * 8) = v2; *(v4u*)(lds + (48 + row) * PK_LDW + ch * 8) = v3;
    __syncthreads();
    pack_emit(p, d0, lds, red);
    pack_emit(p, d1, lds + 32 * PK_LDW, red + 8);
}

DEV void ln_row(float (&v)[8]) {
    float s = 0.f;
#pragma unroll
    for (int j = 0; j < 8; ++j) s += v[j];
    const float mean = wave_sum(s) * (1.f / 512.f);
    float q = 0.f;
#pragma unroll
    for (int j = 0; j < 8; ++j) { v[j] -= mean; q = fmaf(v[j], v[j], q); }
    const float rstd = __builtin_amdgcn_rsqf(wave_sum(q) * (1.f / 512.f) + 1e-5f);
#pragma unroll
    for (int j = 0; j < 8; ++j) v[j] *= rstd;
}
DEV void ln_row_bf(float (&v)[8]) {
    const float piv = __uint_as_float(__builtin_amdgcn_readfirstlane(__float_as_uint(v[0])));
    float s = 0.f, q = 0.f;
#pragma unroll
    for (int j = 0; j < 8; ++j) { const float d = v[j] - piv; s += d; q = fmaf(d, d, q); }
    const float md = wave_sum(s) * (1.f / 512.f);
    const float var = fmaxf(wave_sum(q) * (1.f / 512.f) - md * md, 0.f);
    const float mb = rbf(md + piv);
    const float rden = __builtin_amdgcn_rcpf(rbf(__builtin_amdgcn_sqrtf(rbf(rbf(var) + 1e-5f))));
#pragma unroll
    for (int j = 0; j < 8; ++j) v[j] = rbf(rbf(v[j] - mb) * rden);
}
DEV void ln_row1(float (&v)[8]) {
    const float piv = __uint_as_float(__builtin_amdgcn_readfirstlane(__float_as_uint(v[0])));
    float s = 0.f, q = 0.f;
#pragma unroll
    for (int j = 0; j < 8; ++j) { v[j] -= piv; s += v[j]; q = fmaf(v[j], v[j], q); }
    const float mean = wave_sum(s) * (1.f / 512.f);
    const float var = fmaxf(wave_sum(q) * (1.f / 512.f) - mean * mean, 0.f);
    const float rstd = __builtin_amdgcn_rsqf(var + 1e-5f);
    const float b = -mean * rstd;
#pragma unroll
    for (int j = 0; j < 8; ++j) v[j] = fmaf(v[j], rstd, b);
}
DEV void unpack8(v4u w, float (&v)[8]) {
    v[0] = lo16f(w.x); v[1] = hi16f(w.x); v[2] = lo16f(w.y); v[3] = hi16f(w.y);
    v[4] = lo16f(w.z); v[5] = hi16f(w.z); v[6] = lo16f(w.w); v[7] = hi16f(w.w);
}
DEV v4u pack8(const float (&v)[8]) { v4u o; o.x = pack2(v[0], v[1]); o.y = pack2(v[2], v[3]); o.z = pack2(v[4], v[5]); o.w = pack2(v[6], v[7]); return o; }

constexpr int CTL_LNC = 7168;
constexpr int CTL_STATC = 7168 + 512;
DEV void ln1_block(const Ptrs& p, int blk, char* smem) {
    const int tid = threadIdx.x, lane = tid & 63, wave = tid >> 6;
    GAS unsigned* ctl = (GAS unsigned*)(p.ws + WS_CTL);
    float* red = (float*)smem;
    if (blk >= 128) {
        const int cb = blk - 128;
        const float* cnp = (const float*)(p.ws + WS_CNP) + cb * 64 + lane;
        float s = 0.f;
#pragma unroll 8
        for (int i = 0; i < 32; ++i) s += cnp[(wave * 32 + i) * 512];
        red[tid] = s;
        __syncthreads();
        if (wave == 0) {
            float t = (red[lane] + red[64 + lane]) + (red[128 + lane] + red[192 + lane]);
            unsigned m = wave_umax(__float_as_uint(t) & 0x7fffffffu);
            if (lane == 0) {
                __hip_atomic_store(ctl + CTL_CM2 + cb, m, RLX_AGENT);
                asm volatile("s_waitcnt vmcnt(0)" ::: "memory");
                __hip_atomic_fetch_add(ctl + CTL_STATC, 1u, RLX_AGENT);
            }
        }
        __syncthreads();
        return;
    }
    __amdgpu_buffer_rsrc_t rs = mkrs(p.ws);
    const float* mod = (const float*)(p.ws + WS_MOD);
    float sa[8], ca[8];
#pragma unroll
    for (int j = 0; j < 8; ++j) { sa[j] = mod[lane * 8 + j]; ca[j] = rbf(1.f + mod[512 + lane * 8 + j]); }
    v4u xr[4];
#pragma unroll
    for (int i = 0; i < 4; ++i) xr[i] = *(const v4u*)(p.x + (size_t)(blk * 16 + wave * 4 + i) * 512 + lane * 8);
    unsigned mx = 0;
#pragma unroll
    for (int i = 0; i < 4; ++i) {
        const int row = blk * 16 + wave * 4 + i;
        float v[8];
        unpack8(xr[i], v);
        ln_row_bf(v);
        float ss = 0.f;
#pragma unroll
        for (int j = 0; j < 8; ++j) { v[j] = rbf(rbf(v[j] * ca[j]) + sa[j]); ss = fmaf(v[j], v[j], ss); }
        unsigned sb = __float_as_uint(wave_sum(ss)) & 0x7fffffffu;
        mx = sb > mx ? sb : mx;
        bst4<16>(rs, (unsigned)(WS_H + ((size_t)(row >> 4) * 16 + (lane >> 2)) * 1024 + ((row & 15) + 16 * (lane & 3)) * 16), pack8(v));
    }
    if (lane == 0) red[wave] = __uint_as_float(mx);
    asm volatile("s_waitcnt vmcnt(0)" ::: "memory");
    __syncthreads();
    if (tid == 0) {
        __hip_atomic_fetch_add(ctl + CTL_LNC + (blk >> 2) * 16, 1u, RLX_AGENT);
        unsigned a = 0;
        for (int i = 0; i < 4; ++i) { unsigned o = __float_as_uint(red[i]); a = o > a ? o : a; }
        __hip_atomic_store(ctl + CTL_HMAX + blk, a, RLX_AGENT);
        asm volatile("s_waitcnt vmcnt(0)" ::: "memory");
        __hip_atomic_fetch_add(ctl + CTL_STATC, 1u, RLX_AGENT);
    }
    __syncthreads();
}
template <int MT, int NT, int KS, unsigned SWAPMASK>
DEV void gemm_core_u(__amdgpu_buffer_rsrc_t ra, unsigned aoff, unsigned a_mt_stride, __amdgpu_buffer_rsrc_t rb, const unsigned (&boff)[NT],
                     unsigned b_ks_stride, int rot, v4f (&acc)[MT][NT]) {
    v4u a[3][MT], b[3][NT];
#pragma unroll
    for (int pf = 0; pf < 2; ++pf) {
        const unsigned kr = (unsigned)((pf + rot) & (KS - 1));
#pragma unroll
        for (int n = 0; n < NT; ++n) b[pf][n] = __builtin_amdgcn_raw_buffer_load_b128(rb, boff[n], kr * b_ks_stride, 0);
#pragma unroll
        for (int m = 0; m < MT; ++m) a[pf][m] = __builtin_amdgcn_raw_buffer_load_b128(ra, aoff + m * a_mt_stride, kr * 1024, 16);
    }
#pragma unroll
    for (int ks = 0; ks < KS; ++ks) {
        if (ks + 2 < KS) {
            const unsigned kr = (unsigned)((ks + 2 + rot) & (KS - 1));
#pragma unroll
            for (int n = 0; n < NT; ++n) b[(ks + 2) % 3][n] = __builtin_amdgcn_raw_buffer_load_b128(rb, boff[n], kr * b_ks_stride, 0);
#pragma unroll
            for (int m = 0; m < MT; ++m) a[(ks + 2) % 3][m] = __builtin_amdgcn_raw_buffer_load_b128(ra, aoff + m * a_mt_stride, kr * 1024, 16);
        }
        __builtin_amdgcn_sched_barrier(0);
#pragma unroll
        for (int m = 0; m < MT; ++m)
#pragma unroll
            for (int n = 0; n < NT; ++n)
                acc[m][n] = ((SWAPMASK >> n) & 1) ? mfma16(b[ks % 3][n], a[ks % 3][m], acc[m][n]) : mfma16(a[ks % 3][m], b[ks % 3][n], acc[m][n]);
#pragma unroll
        for (int m = 0; m < MT; ++m) asm volatile("" : "+a"(acc[m][0]), "+a"(acc[m][NT - 1]));
        __builtin_amdgcn_sched_barrier(0);
    }
}

template <int AUXS>
DEV void task_qkv(const Ptrs& p, int rb, int h, int wave, int lane) {
    __amdgpu_buffer_rsrc_t rs = mkrs(p.ws);
    v4f acc[4][6];
#pragma unroll
    for (int m = 0; m < 4; ++m)
#pragma unroll
        for (int n = 0; n < 6; ++n) acc[m][n] = (v4f){0, 0, 0, 0};
    unsigned boff[6];
#pragma unroll
    for (int n = 0; n < 6; ++n) boff[n] = (unsigned)(WS_WQKV + (size_t)(h * 3 + (n >> 1)) * 131072 + (2 * wave + (n & 1)) * 1024 + lane * 16);
    gemm_core_u<4, 6, 16, 0xF>(rs, (unsigned)(WS_H + (size_t)rb * 4 * 16384 + lane * 16), 16384, rs, boff, 8192, rb * 3, acc);
    const int g = lane >> 4, l15 = lane & 15;
#pragma unroll
    for (int m = 0; m < 4; ++m) {
        const int seg = rb * 2 + (m >> 1), tile = m & 1;
#pragma unroll
        for (int pq = 0; pq < 2; ++pq) {
            const v4f oa = acc[m][2 * pq], ob = acc[m][2 * pq + 1];
            unsigned a0 = pack2(oa[0], oa[1]), a1 = pack2(oa[2], oa[3]), b0 = pack2(ob[0], ob[1]), b1 = pack2(ob[2], ob[3]);
            auto t0 = __builtin_amdgcn_permlane16_swap(a0, b0, false, false);
            auto t1 = __builtin_amdgcn_permlane16_swap(a1, b1, false, false);
            v4u ov = {t0[0], t1[0], t0[1], t1[1]};
            unsigned off = (unsigned)((pq == 0 ? WS_Q : WS_K) + ((size_t)((seg * 32 + h) * 2 + tile) * 4 + wave) * 1024 + (l15 + 16 * (2 * (g & 1) + (g >> 1))) * 16);
            bst4<AUXS>(rs, off, ov);
        }
    }
#pragma unroll
    for (int sg = 0; sg < 2; ++sg)
#pragma unroll
        for (int i = 0; i < 2; ++i) {
            const v4f a = acc[2 * sg][4 + i], b = acc[2 * sg + 1][4 + i];
            v4u o; o.x = pack2(a[0], a[1]); o.y = pack2(a[2], a[3]); o.z = pack2(b[0], b[1]); o.w = pack2(b[2], b[3]);
            bst4<AUXS>(rs, (unsigned)(WS_V + ((size_t)((rb * 2 + sg) * 32 + h) * 8 + 2 * wave + i) * 1024 + lane * 16), o);
        }
}

template <int AUXL, int AUXS, class WAITF>
DEV void att_half(const Ptrs& p, int seg, int h, int qt, int lane, WAITF wait_mem) {
    __amdgpu_buffer_rsrc_t rs = mkrs(p.ws);
    const int g = lane >> 4, l15 = lane & 15;
    const unsigned pair = (unsigned)(seg * 32 + h);
    v4u q[4], k0[4], k1[4], km[4], vf[8]; v2u vm[8];
#pragma unroll
    for (int ks = 0; ks < 4; ++ks) {
        q[ks] = bld4<AUXL>(rs, (unsigned)WS_Q + pair * 8192 + (qt * 4 + ks) * 1024 + lane * 16);
        k0[ks] = bld4<AUXL>(rs, (unsigned)WS_K + pair * 8192 + ks * 1024 + lane * 16);
        k1[ks] = bld4<AUXL>(rs, (unsigned)WS_K + pair * 8192 + (4 + ks) * 1024 + lane * 16);
    }
#pragma unroll
    for (int et = 0; et < 8; ++et) vf[et] = bld4<AUXL>(rs, (unsigned)WS_V + pair * 8192 + et * 1024 + lane * 16);
    wait_mem();
#pragma unroll
    for (int ks = 0; ks < 4; ++ks) km[ks] = bld4<AUXL>(rs, (unsigned)WS_KM + pair * 4096 + ks * 1024 + lane * 16);
#pragma unroll
    for (int et = 0; et < 8; ++et) vm[et] = bld2<AUXL>(rs, (unsigned)WS_VM + pair * 2048 + et * 256 + (lane & 31) * 8);
    const float sc = 0.08838834764831845f * 1.4426950408889634f;
    v4f sm = {0, 0, 0, 0}, s0 = {0, 0, 0, 0}, s1 = {0, 0, 0, 0};
#pragma unroll
    for (int ks = 0; ks < 4; ++ks) { sm = mfma16(km[ks], q[ks], sm); s0 = mfma16(k0[ks], q[ks], s0); s1 = mfma16(k1[ks], q[ks], s1); }
    const int qi = 16 * qt + l15;
    const float NEG = -3.0e38f;
    float mx = NEG;
#pragma unroll
    for (int r = 0; r < 4; ++r) {
        sm[r] = (g < 2) ? sm[r] * sc : NEG;
        s0[r] = (4 * g + r <= qi) ? s0[r] * sc : NEG;
        s1[r] = (16 + 4 * g + r <= qi) ? s1[r] * sc : NEG;
        mx = fmaxf(mx, fmaxf(sm[r], fmaxf(s0[r], s1[r])));
    }
    mx = xmax16_32(mx);
    float sum = 0.f;
#pragma unroll
    for (int r = 0; r < 4; ++r) {
        sm[r] = __builtin_amdgcn_exp2f(sm[r] - mx); s0[r] = __builtin_amdgcn_exp2f(s0[r] - mx); s1[r] = __builtin_amdgcn_exp2f(s1[r] - mx);
        sum += sm[r] + s0[r] + s1[r];
    }
    sum = xsum16_32(sum);
    const float inv = 1.f / sum;
    v4u p0, p1;
    p0.x = pack2(sm[0] * inv, sm[1] * inv); p0.y = pack2(sm[2] * inv, sm[3] * inv); p0.z = 0; p0.w = 0;
    p1.x = pack2(s0[0] * inv, s0[1] * inv); p1.y = pack2(s0[2] * inv, s0[3] * inv);
    p1.z = pack2(s1[0] * inv, s1[1] * inv); p1.w = pack2(s1[2] * inv, s1[3] * inv);
#pragma unroll
    for (int e2 = 0; e2 < 4; ++e2) {
        v4f oa = {0, 0, 0, 0}, ob = {0, 0, 0, 0};
        oa = mfma16((v4u){vm[2 * e2].x, vm[2 * e2].y, 0, 0}, p0, oa); oa = mfma16(vf[2 * e2], p1, oa);
        ob = mfma16((v4u){vm[2 * e2 + 1].x, vm[2 * e2 + 1].y, 0, 0}, p0, ob); ob = mfma16(vf[2 * e2 + 1], p1, ob);
        unsigned a0 = pack2(oa[0], oa[1]), a1 = pack2(oa[2], oa[3]), b0 = pack2(ob[0], ob[1]), b1 = pack2(ob[2], ob[3]);
        auto t0 = __builtin_amdgcn_permlane16_swap(a0, b0, false, false);
        auto t1 = __builtin_amdgcn_permlane16_swap(a1, b1, false, false);
        v4u ov = {t0[0], t1[0], t0[1], t1[1]};
        unsigned off = (unsigned)(WS_O + ((size_t)(seg * 2 + qt) * 128 + h * 4 + e2) * 1024 + (l15 + 16 * (2 * (g & 1) + (g >> 1))) * 16);
        bst4<AUXS>(rs, off, ov);
    }
}

DEV float gelu_tanh(float x) {
    float u2 = 1.5957691216057308f * (x + 0.044715f * x * x * x);
    return x / (1.f + __expf(-u2));
}
constexpr int HL_MEMA = 0, HL_QT = HL_MEMA + 8192, HL_KT = HL_QT + 2048, HL_OA = HL_KT + 2048, HL_KTOK = HL_OA + 2048, HL_P0 = HL_KTOK + 8192, HL_P1 = HL_P0 + 512,
              HL_INV = HL_P1 + 1024, HL_VM = HL_INV + 256, HL_SC = HL_VM + 2048, HL_MX = HL_SC + 16, HL_WV = HL_SC + 64, HL_END = HL_WV + 131072;
constexpr int NHB = 64;
#define SPIN_CAP (1u << 22)
#define RANK_DIV 3
#define LBAR() asm volatile("s_waitcnt lgkmcnt(0)\n\ts_barrier" ::: "memory")
#define SB() __builtin_amdgcn_sched_barrier(0)

DEV v4u lds_rd4(const char* smem, int off) { return *(const v4u*)(smem + off); }
DEV v2u lds_tr(const char* smem, int off) {
    typedef short v4i16_t __attribute__((ext_vector_type(4)));
    return __builtin_bit_cast(v2u, __builtin_amdgcn_ds_read_tr16_b64_v4i16((LAS v4i16_t*)(smem + off)));
}
DEV v4f vzero() { v4f z = {0, 0, 0, 0}; asm volatile("" : "+v"(z)); return z; }
DEV v4f vinit16() { v4f z = {16.f, 0.f, 16.f, 0.f}; asm volatile("" : "+v"(z)); return z; }
DEV float max3a(float a, float b, float c) { float r; asm("v_max3_f32 %0, %1, %2, %3" : "=v"(r) : "v"(a), "v"(b), "v"(c)); return r; }
DEV float max2a(float a, float b) { float r; asm("v_max_f32 %0, %1, %2" : "=v"(r) : "v"(a), "v"(b)); return r; }
DEV float xmax_fast(float x) {
    auto a = __builtin_amdgcn_permlane16_swap(__float_as_uint(x), __float_as_uint(x), false, false);
    x = max2a(__uint_as_float(a[0]), __uint_as_float(a[1]));
    auto b = __builtin_amdgcn_permlane32_swap(__float_as_uint(x), __float_as_uint(x), false, false);
    return max2a(__uint_as_float(b[0]), __uint_as_float(b[1]));
}
template <int CTRL> DEV unsigned dpp_u(unsigned x) { return (unsigned)__builtin_amdgcn_update_dpp(0, (int)x, CTRL, 0xf, 0xf, false); }
DEV unsigned umax2(unsigned a, unsigned b) { return a > b ? a : b; }
DEV unsigned wave_umax_dpp(unsigned x) {
    x = umax2(x, dpp_u<0x128>(x)); x = umax2(x, dpp_u<0x124>(x)); x = umax2(x, dpp_u<0x122>(x)); x = umax2(x, dpp_u<0x121>(x));
    auto a = __builtin_amdgcn_permlane16_swap(x, x, false, false); x = umax2(a[0], a[1]);
    auto b = __builtin_amdgcn_permlane32_swap(x, x, false, false); return umax2(b[0], b[1]);
}
DEV v8u cat8(v4u a, v4u b) { return (v8u){a.x, a.y, a.z, a.w, b.x, b.y, b.z, b.w}; }
DEV int cvt_rpi(float x) { int r; asm("v_cvt_rpi_i32_f32 %0, %1" : "=v"(r) : "v"(x)); return r; }
DEV v4f fold4(v4f a, v4f b) {
    v4f x, y;
#pragma unroll
    for (int r = 0; r < 4; ++r) {
        auto t = __builtin_amdgcn_permlane32_swap(__float_as_uint(a[r]), __float_as_uint(b[r]), false, false);
        x[r] = __uint_as_float(t[0]); y[r] = __uint_as_float(t[1]);
    }
    return x + y;
}

DEV void head_main(const Ptrs& p, int hb, char* smem) {
    const int tid = threadIdx.x, lane = tid & 63, wave = __builtin_amdgcn_readfirstlane(tid >> 6);
    const int g = lane >> 4, l15 = lane & 15;
    const int h = hb & 31, hf = hb >> 5;
    __amdgpu_buffer_rsrc_t rs = mkrs(p.ws);
    constexpr int AX = 16;
    v8u WQ[2][8], WK[2][8], WO[4][2];
#pragma unroll
    for (int i = 0; i < 2; ++i)
#pragma unroll
        for (int t = 0; t < 8; ++t) {
            unsigned oq = (unsigned)(WS_WQKV + (size_t)(h * 3 + 0) * 131072 + (2 * t) * 8192 + (2 * wave + i) * 1024 + lane * 16);
            WQ[i][t] = cat8(bld4<0>(rs, oq), bld4<0>(rs, oq + 8192));
            WK[i][t] = cat8(bld4<0>(rs, oq + 131072), bld4<0>(rs, oq + 131072 + 8192));
            v4u z0 = bld4<0>(rs, oq + 262144), z1 = bld4<0>(rs, oq + 262144 + 8192);
            *(v4u*)(smem + HL_WV + (2 * wave + i) * 16384 + (t * 2 + 0) * 1024 + lane * 16) = z0;
            *(v4u*)(smem + HL_WV + (2 * wave + i) * 16384 + (t * 2 + 1) * 1024 + lane * 16) = z1;
        }
#pragma unroll
    for (int n = 0; n < 4; ++n)
#pragma unroll
        for (int t = 0; t < 2; ++t) {
            unsigned oo = (unsigned)(WS_WO + (size_t)(h * 4 + 2 * t) * 32768 + (hf * 16 + wave * 4 + n) * 1024 + lane * 16);
            WO[n][t] = cat8(bld4<0>(rs, oo), bld4<0>(rs, oo + 32768));
        }
    unsigned mx0 = 0;
    for (int idx = tid; idx < 2048; idx += 256) {
        int m = idx >> 8, c = (idx & 255) * 2;
        unsigned w = *(const unsigned*)(p.mem0 + m * 512 + c);
        { unsigned u0 = (w << 16) & 0x7fffffffu, u1 = w & 0x7fff0000u; u0 = u0 > u1 ? u0 : u1; mx0 = mx0 > u0 ? mx0 : u0; }
        *(unsigned*)(smem + HL_MEMA + (c >> 6) * 1024 + (m + 8 * ((c >> 1) & 1) + 16 * ((c >> 4) & 3)) * 16 + ((c >> 2) & 3) * 4) = w;
    }
    mx0 = wave_umax(mx0);
    if (lane == 0) *(unsigned*)(smem + HL_MX + wave * 4) = mx0;
    float hmax, cw;
    {
        GAS unsigned* cg = (GAS unsigned*)(p.ws + WS_CTL);
        { unsigned spins = 0; while ((unsigned)__builtin_amdgcn_readfirstlane(__hip_atomic_load(cg + CTL_STATC, RLX_AGENT)) < 136u && ++spins < SPIN_CAP) __builtin_amdgcn_s_sleep(2); asm volatile("" ::: "memory"); }
        unsigned a = __hip_atomic_load(cg + CTL_HMAX + lane, RLX_AGENT), b2 = __hip_atomic_load(cg + CTL_HMAX + 64 + lane, RLX_AGENT);
        hmax = __uint_as_float(__builtin_amdgcn_readfirstlane(__float_as_uint(sqrtf(__uint_as_float(wave_umax(a > b2 ? a : b2))))));
        const float cm2 = __uint_as_float(wave_umax(__hip_atomic_load(cg + CTL_CM2 + (lane & 7), RLX_AGENT)));
        const float* fvp = (const float*)(p.ws + WS_FVP);
        float f = 0.f;
#pragma unroll
        for (int i = 0; i < 8; ++i) f += fvp[i * 64 + lane];
        cw = __uint_as_float(__builtin_amdgcn_readfirstlane(__float_as_uint(1.27f * sqrtf(wave_sum(f)) * sqrtf(cm2))));
    }
    const int sidx = (l15 < 8) ? 0x4444 : 0xEEEE;
    v4u VT[2];
    unsigned long long rmask = 0;
    GAS unsigned* ctlg = (GAS unsigned*)(p.ws + WS_CTL);
    { unsigned spins = 0; while (__builtin_amdgcn_readfirstlane(__hip_atomic_load(ctlg + 1024 + h, RLX_AGENT)) == 0 && ++spins < SPIN_CAP) __builtin_amdgcn_s_sleep(2); asm volatile("" ::: "memory"); }
    {
        const unsigned pr = (unsigned)h;
        v4u k0 = bld4<AX>(rs, (unsigned)WS_K + pr * 8192 + tid * 16), k1 = bld4<AX>(rs, (unsigned)WS_K + pr * 8192 + 4096 + tid * 16);
        *(v4u*)(smem + HL_KTOK + tid * 16) = k0; *(v4u*)(smem + HL_KTOK + 4096 + tid * 16) = k1;
        VT[0] = bld4<AX>(rs, (unsigned)WS_V + pr * 8192 + (2 * wave) * 1024 + lane * 16);
        VT[1] = bld4<AX>(rs, (unsigned)WS_V + pr * 8192 + (2 * wave + 1) * 1024 + lane * 16);
    }
    __syncthreads();
    asm volatile("" : "+v"(VT[0]), "+v"(VT[1]));
    const float sc = 0.08838834764831845f * 1.4426950408889634f;
    const char* lb = smem + lane * 16;
    const char* lv = smem + (wave == 0 ? HL_KTOK : HL_WV + (wave == 3 ? 5 : (wave == 2 ? 3 : 0)) * 16384) + lane * 16;
    const char* ltr = smem + ((l15 >> 2) + 8 * g) * 16 + (lane & 1) * 8;
    char* lqw = smem + (32 * wave + 16 * (lane >> 5) + l15) * 16 + (g & 1) * 8;
    char* low = smem + HL_OA + (wave >> 1) * 1024 + ((l15 & 7) + 16 * (2 * (wave & 1) + (l15 >> 3))) * 16 + g * 4;
    char* lmw = smem + HL_MEMA + (wave >> 1) * 1024 + (4 * ((lane >> 3) & 1) + 2 * (lane >> 5) + 8 * (lane & 1) + 16 * (2 * (wave & 1) + ((lane >> 4) & 1))) * 16 + ((lane >> 1) & 3) * 4;
    for (int s = 0; s < NSEG; ++s) {
#pragma unroll
        for (int i = 0; i < 2; ++i)
#pragma unroll
            for (int t = 0; t < 8; ++t) { asm volatile("" : "+a"(WQ[i][t])); if (t < 7) asm volatile("" : "+a"(WK[i][t])); }
        v4u a[8];
#pragma unroll
        for (int t = 0; t < 8; ++t) { a[t] = lds_rd4(lb, HL_MEMA + t * 1024); SB(); }
        v4u x[3][3][2];
        SB();
        v4f aq0 = vzero(), aq1 = vzero(), ak0 = vzero(), ak1 = vzero();
#pragma unroll
        for (int t = 0; t < 8; ++t) {
            aq0 = smfmac(a[t], WQ[0][t], aq0, sidx); aq1 = smfmac(a[t], WQ[1][t], aq1, sidx);
            ak0 = smfmac(a[t], WK[0][t], ak0, sidx); ak1 = smfmac(a[t], WK[1][t], ak1, sidx);
            asm volatile("" : "+v"(aq0), "+v"(aq1), "+v"(ak0), "+v"(ak1));
            if (t < 6) {
#pragma unroll
                for (int j = 0; j < 3; ++j) x[t >> 1][j][t & 1] = lds_rd4(lv, j * 16384 + t * 1024);
            }
            SB();
        }
        v2u sv;
            {
                const v4u mq = *(const v4u*)(smem + HL_MX);
                unsigned m1 = mq.x > mq.y ? mq.x : mq.y, m2 = mq.z > mq.w ? mq.z : mq.w;
                const float mm = __uint_as_float(m1 > m2 ? m1 : m2);
                const float Bv = cw * fmaxf(hmax, 22.9f * mm) + (mm - mm);
                int eb = (int)((__float_as_uint(Bv) >> 23) & 255);
                const bool bad = eb == 255;
                eb = eb < 64 ? 64 : (eb > 254 ? 254 : eb);
                sv.x = (unsigned)(284 - eb) << 23; sv.y = bad ? 0x7fc00000u : ((unsigned)(eb - 30) << 23);
            }
        {
            v4f fq = fold4(aq0, aq1), fk = fold4(ak0, ak1);
            v2u oq, ok;
            oq.x = pack2(fq[0], fq[1]); oq.y = pack2(fq[2], fq[3]); ok.x = pack2(fk[0], fk[1]); ok.y = pack2(fk[2], fk[3]);
            *(v2u*)(lqw + HL_QT) = oq;
            *(v2u*)(lqw + HL_KT) = ok;
        }
        LBAR();
        if (wave == 0) {
            v4u qf[4], kf[4];
            const v4u t6 = lds_rd4(lb, HL_KTOK + 6144), t7 = lds_rd4(lb, HL_KTOK + 7168);
#pragma unroll
            for (int ks = 0; ks < 4; ++ks) {
                v2u qa = lds_tr(ltr, HL_QT + ks * 512), qb = lds_tr(ltr, HL_QT + ks * 512 + 64);
                v2u ka = lds_tr(ltr, HL_KT + ks * 512), kb = lds_tr(ltr, HL_KT + ks * 512 + 64);
                qf[ks] = (v4u){qa.x, qa.y, qb.x, qb.y}; kf[ks] = (v4u){ka.x, ka.y, kb.x, kb.y};
            }
            v4f sm = {0, 0, 0, 0}, s0 = sm, s1 = sm;
#pragma unroll
            for (int ks = 0; ks < 4; ++ks) { s0 = mfma16(x[ks >> 1][0][ks & 1], qf[ks], s0); s1 = mfma16(ks < 2 ? x[2][0][ks & 1] : (ks == 2 ? t6 : t7), qf[ks], s1); sm = mfma16(kf[ks], qf[ks], sm); }
            float mx = max3a(max3a(sm[0], sm[1], sm[2]), max3a(sm[3], s0[0], s0[1]), max3a(max3a(s0[2], s0[3], s1[0]), max3a(s1[1], s1[2], s1[3]), sm[0]));
            mx = xmax_fast(mx) * sc;
#pragma unroll
            for (int r = 0; r < 4; ++r) {
                sm[r] = __builtin_amdgcn_exp2f(fmaf(sm[r], sc, -mx)); s0[r] = __builtin_amdgcn_exp2f(fmaf(s0[r], sc, -mx)); s1[r] = __builtin_amdgcn_exp2f(fmaf(s1[r], sc, -mx));
            }
            v2u p0; v4u p1;
            p0.x = pack2(sm[0], sm[1]); p0.y = pack2(sm[2], sm[3]);
            p1.x = pack2(s0[0], s0[1]); p1.y = pack2(s0[2], s0[3]); p1.z = pack2(s1[0], s1[1]); p1.w = pack2(s1[2], s1[3]);
            *(v2u*)(smem + HL_P0 + lane * 8) = p0;
            *(v4u*)(smem + HL_P1 + lane * 16) = p1;
            v4f t = s0 + s1;
            float m4 = (sm[0] + sm[1]) + (sm[2] + sm[3]);
            float sum = (t[0] + t[1]) + (t[2] + t[3]) + (g < 2 ? m4 : 0.f);
            sum = xsum16_32(sum);
            *(float*)(smem + HL_INV + lane * 4) = __builtin_amdgcn_rcpf(sum);
            if (hf == 0) {
#pragma unroll
                for (int ks = 0; ks < 4; ++ks) bst4<AX>(rs, (unsigned)(WS_KM + (size_t)(s * 32 + h) * 4096 + ks * 1024 + lane * 16), kf[ks]);
            }
        } else {
            v4f av0 = vzero(), av1 = vzero(), av2 = vzero();
#pragma unroll
            for (int t = 0; t < 8; ++t) {
                const int b = t % 3;
                av0 = smfmac(a[t], cat8(x[b][0][0], x[b][0][1]), av0, sidx); av1 = smfmac(a[t], cat8(x[b][1][0], x[b][1][1]), av1, sidx);
                av2 = smfmac(a[t], cat8(x[b][2][0], x[b][2][1]), av2, sidx);
                asm volatile("" : "+v"(av0), "+v"(av1), "+v"(av2));
                SB();
                if (t + 3 < 8) {
#pragma unroll
                    for (int j = 0; j < 3; ++j) { x[b][j][0] = lds_rd4(lv, j * 16384 + (t + 3) * 2048); x[b][j][1] = lds_rd4(lv, j * 16384 + (t + 3) * 2048 + 1024); }
                    SB();
                }
            }
            const int tb = wave == 3 ? 5 : (wave - 1) * 3;
            v4f fv = fold4(av1, av2), fw = fold4(av0, av0);
            v2u vp; vp.x = pack2(fv[0], fv[1]); vp.y = pack2(fv[2], fv[3]);
            v2u wp; wp.x = pack2(fw[0], fw[1]); wp.y = pack2(fw[2], fw[3]);
            *(v2u*)(smem + HL_VM + (tb + 1) * 256 + lane * 8) = vp;
            *(v2u*)(smem + HL_VM + tb * 256 + (lane & 31) * 8) = wp;
            if (hf == 0) {
                bst2<AX>(rs, (unsigned)(WS_VM + (size_t)(s * 32 + h) * 2048 + (tb + 1) * 256 + lane * 8), vp);
                bst2<AX>(rs, (unsigned)(WS_VM + (size_t)(s * 32 + h) * 2048 + tb * 256 + (lane & 31) * 8), wp);
            }
        }
        if (s == NSEG - 1) {
            asm volatile("s_waitcnt vmcnt(0)" ::: "memory"); LBAR();
            if (tid == 0 && hf == 0) __hip_atomic_store(ctlg + 2048 + s * 32 + h, 1u, RLX_AGENT);
            break;
        }
        LBAR();
        float inv64;
        {
            v2u p0 = *(const v2u*)(smem + HL_P0 + lane * 8);
            v4u p1 = lds_rd4(lb, HL_P1);
            v2u m0 = *(const v2u*)(smem + HL_VM + wave * 512 + (lane & 31) * 8), m1 = *(const v2u*)(smem + HL_VM + wave * 512 + 256 + (lane & 31) * 8);
            const float inv = *(const float*)(smem + HL_INV + lane * 4);
            inv64 = __uint_as_float(sv.y);
            v4f o0 = {0, 0, 0, 0}, o1 = o0;
            o0 = mfma16(VT[0], p1, o0); o1 = mfma16(VT[1], p1, o1);
            v4u pz = {lane < 32 ? p0.x : 0u, lane < 32 ? p0.y : 0u, 0, 0};
            o0 = mfma16((v4u){m0.x, m0.y, 0, 0}, pz, o0); o1 = mfma16((v4u){m1.x, m1.y, 0, 0}, pz, o1);
            v4f o = (l15 < 8) ? o0 : o1;
            const float is = inv * __uint_as_float(sv.x);
            *(unsigned*)low = pack2(o[0] * is, o[1] * is);
            *(unsigned*)(low + 128) = pack2(o[2] * is, o[3] * is);
        }
        LBAR();
        unsigned rank_v;
        {
            v4u a0 = lds_rd4(lb, HL_OA), a1 = lds_rd4(lb, HL_OA + 1024);
            GAS unsigned long long* accp = (GAS unsigned long long*)(p.ws + WS_ACC) + (size_t)s * 4096 + (hf * 4 + wave) * 512 + lane;
            v4f c[4];
#pragma unroll
            for (int n = 0; n < 4; ++n) { c[n] = vinit16(); c[n] = smfmac(a0, WO[n][0], c[n], sidx); }
#pragma unroll
            for (int n = 0; n < 4; ++n) c[n] = smfmac(a1, WO[n][1], c[n], sidx);
#pragma unroll
            for (int i = 0; i < 2; ++i) {
                v4f f = fold4(c[2 * i], c[2 * i + 1]);
#pragma unroll
                for (int r = 0; r < 2; ++r) {
                    const unsigned lo = ((unsigned)cvt_rpi(f[2 * r]) & ~63u) | 1u; const unsigned hi = (unsigned)(cvt_rpi(f[2 * r + 1]) + ((int)lo >> 31));
                    const unsigned long long old = __hip_atomic_fetch_add(accp + i * 128 + r * 64, ((unsigned long long)hi << 32) | lo, RLX_AGENT);
                    if (i == 1 && r == 1) rank_v = (unsigned)old;
                }
            }
        }
        SB();
        v4u kn0, kn1;
        {
            const int sn = s + 1;
            const unsigned pr = (unsigned)(sn * 32 + h);
            if (!((rmask >> (sn >> 1)) & 1ull)) {
                unsigned spins = 0;
                do { rmask = __ballot(__hip_atomic_load(ctlg + 1024 + (lane & 31) * 32 + h, RLX_AGENT) != 0); } while (!((rmask >> (sn >> 1)) & 1ull) && ++spins < SPIN_CAP);
                asm volatile("" ::: "memory");
            }
            kn0 = bld4<AX>(rs, (unsigned)WS_K + pr * 8192 + tid * 16); kn1 = bld4<AX>(rs, (unsigned)WS_K + pr * 8192 + 4096 + tid * 16);
            VT[0] = bld4<AX>(rs, (unsigned)WS_V + pr * 8192 + (2 * wave) * 1024 + lane * 16);
            VT[1] = bld4<AX>(rs, (unsigned)WS_V + pr * 8192 + (2 * wave + 1) * 1024 + lane * 16);
        }
        {
            const int rank = __builtin_amdgcn_readfirstlane(rank_v) & 63;
            for (int z = (31 - rank) / RANK_DIV; z > 0; --z) __builtin_amdgcn_s_sleep(1);
        }
        {
            v4u r[4];
            unsigned spins = 0, mxb;
            const unsigned aoff = (unsigned)(WS_ACC + (size_t)s * 32768 + (tid >> 7) * 4096 + (tid & 127) * 16);
            const float off = -32.f * inv64;
            for (;;) {
                asm volatile("" ::: "memory");
#pragma unroll
                for (int i = 0; i < 4; ++i) r[i] = bld4<16>(rs, aoff + i * 8192);
                unsigned all = 32u;
                mxb = 0;
#pragma unroll
                for (int i = 0; i < 4; ++i) {
                    all &= r[i].x & r[i].z;
                    float a0 = fmaf((float)(int)r[i].x, inv64, off), a1 = fmaf((float)(int)r[i].z, inv64, off);
                    float b0 = (float)((int)r[i].y - ((int)r[i].x >> 31)) * inv64, b1 = (float)((int)r[i].w - ((int)r[i].z >> 31)) * inv64;
                    { unsigned u0 = __float_as_uint(a0) & 0x7fffffffu, u1 = __float_as_uint(a1) & 0x7fffffffu, u2 = __float_as_uint(b0) & 0x7fffffffu, u3 = __float_as_uint(b1) & 0x7fffffffu;
                      u0 = u0 > u1 ? u0 : u1; u2 = u2 > u3 ? u2 : u3; u0 = u0 > u2 ? u0 : u2; mxb = mxb > u0 ? mxb : u0; }
                    *(unsigned*)(lmw + i * 2048) = pack2(a0, a1);
                    *(unsigned*)(lmw + i * 2048 + 16) = pack2(b0, b1);
                }
                if (__all(all != 0) || ++spins > SPIN_CAP) break;
            }
            asm volatile("" : "+v"(VT[0]), "+v"(VT[1]));
            *(v4u*)(smem + HL_KTOK + tid * 16) = kn0; *(v4u*)(smem + HL_KTOK + 4096 + tid * 16) = kn1;
            mxb = wave_umax_dpp(mxb);
            if (lane == 0) *(unsigned*)(smem + HL_MX + wave * 4) = mxb;
        }
        LBAR();
        if (tid == 0 && hf == 0) __hip_atomic_store(ctlg + 2048 + s * 32 + h, 1u, RLX_AGENT);
    }
}
constexpr int CTL_QKVF = 1024;
constexpr int CTL_KVM  = 2048;
constexpr int CTL_ATTC = 4096;
constexpr int CTL_WOC  = CTL_ATTC + 1024;
constexpr int CTL_M1C  = CTL_WOC + 1024;
constexpr int NWORK = 256 - NHB;

DEV unsigned ld_ctl(const Ptrs& p, int idx) { return __hip_atomic_load((GAS unsigned*)(p.ws + WS_CTL) + idx, RLX_AGENT); }
DEV void st_ctl(const Ptrs& p, int idx, unsigned v) { __hip_atomic_store((GAS unsigned*)(p.ws + WS_CTL) + idx, v, RLX_AGENT); }
DEV void add_ctl(const Ptrs& p, int idx) { __hip_atomic_fetch_add((GAS unsigned*)(p.ws + WS_CTL) + idx, 1u, RLX_AGENT); }
#define BAR_ONLY() asm volatile("s_waitcnt lgkmcnt(0)\n\ts_barrier" ::: "memory")
#define DRAIN() asm volatile("s_waitcnt vmcnt(0)" ::: "memory")
#define SLOW_NAP 64
DEV void nap(bool fast) { if (fast) __builtin_amdgcn_s_sleep(3); else __builtin_amdgcn_s_sleep(SLOW_NAP); }
DEV void wait_counter(const Ptrs& p, int idx, unsigned want, int wave, bool fast) {
    if (wave == 0) {
        unsigned spins = 0;
        while ((unsigned)__builtin_amdgcn_readfirstlane(ld_ctl(p, idx)) < want && ++spins < SPIN_CAP) nap(fast);
    }
    BAR_ONLY();
}
DEV void signal_counter(const Ptrs& p, int idx) {
    DRAIN();
    BAR_ONLY();
    if (threadIdx.x == 0) add_ctl(p, idx);
}

DEV void att_task(const Ptrs& p, int seg, int slot, int wave, int lane) {
    const int h = slot * 2 + (wave >> 1);
    unsigned spins = 0;
    while (__builtin_amdgcn_readfirstlane(ld_ctl(p, CTL_QKVF + (seg >> 1) * 32 + h)) == 0 && ++spins < SPIN_CAP) nap(false);
    asm volatile("" ::: "memory");
    att_half<16, 16>(p, seg, h, wave & 1, lane, [&]() {
        unsigned sp = 0;
        while (__builtin_amdgcn_readfirstlane(ld_ctl(p, CTL_KVM + seg * 32 + h)) == 0 && ++sp < SPIN_CAP) nap(seg >= NSEG - 4);
        asm volatile("" ::: "memory");
    });
    signal_counter(p, CTL_ATTC + seg * 16);
}

DEV v4f ksplit_reduce(v4f (&acc)[2][2], char* smem, int wave, int lane) {
#pragma unroll
    for (int m = 0; m < 2; ++m)
#pragma unroll
        for (int n = 0; n < 2; ++n) *(v4f*)(smem + ((wave * 4 + m * 2 + n) * 64 + lane) * 16) = acc[m][n];
    BAR_ONLY();
    v4f r = *(const v4f*)(smem + ((0 * 4 + wave) * 64 + lane) * 16);
#pragma unroll
    for (int w = 1; w < 4; ++w) r += *(const v4f*)(smem + ((w * 4 + wave) * 64 + lane) * 16);
    return r;
}

DEV void worker_wo(const Ptrs& p, int grp, int j, int wave, int lane, char* smem) {
    __amdgpu_buffer_rsrc_t rs = mkrs(p.ws);
    v4u B[32][2];
#pragma unroll
    for (int ks = 0; ks < 32; ++ks)
#pragma unroll
        for (int n = 0; n < 2; ++n) B[ks][n] = bld4<0>(rs, (unsigned)(WS_WO + (size_t)(wave * 32 + ((ks + 2 * j) & 31)) * 32768 + (2 * j + n) * 1024 + lane * 16));
    const int g = lane >> 4, l15 = lane & 15;
    const int c = (2 * j + (wave & 1)) * 16 + 4 * g;
    const v4f ga = *(const v4f*)((const float*)(p.ws + WS_MOD) + 1024 + c);
    for (int seg = grp; seg < NSEG; seg += 4) {
        const int row = (seg * 2 + (wave >> 1)) * 16 + l15;
        const v2u xv = *(const v2u*)(p.x + (size_t)row * 512 + c);
#pragma unroll
        for (int ks = 0; ks < 30; ++ks) asm volatile("" : "+a"(B[ks][0]), "+a"(B[ks][1]));
        att_task(p, seg, j, wave, lane);
        wait_counter(p, CTL_ATTC + seg * 16, 16, wave, true);
        v4f acc[2][2] = {{{0, 0, 0, 0}, {0, 0, 0, 0}}, {{0, 0, 0, 0}, {0, 0, 0, 0}}};
        const unsigned aoff = (unsigned)(WS_O + (size_t)seg * 2 * 131072 + wave * 32768 + lane * 16);
        v4u A[3][8][2];
#pragma unroll
        for (int bt = 0; bt < 2; ++bt)
#pragma unroll
            for (int k = 0; k < 8; ++k) { const unsigned ko = (unsigned)((bt * 8 + k + 2 * j) & 31) * 1024; A[bt][k][0] = bld4<16>(rs, aoff + ko); A[bt][k][1] = bld4<16>(rs, aoff + 131072 + ko); }
#pragma unroll
        for (int bt = 0; bt < 4; ++bt) {
            if (bt + 2 < 4) {
#pragma unroll
                for (int k = 0; k < 8; ++k) { const unsigned ko = (unsigned)(((bt + 2) * 8 + k + 2 * j) & 31) * 1024; A[(bt + 2) % 3][k][0] = bld4<16>(rs, aoff + ko); A[(bt + 2) % 3][k][1] = bld4<16>(rs, aoff + 131072 + ko); }
            }
            __builtin_amdgcn_sched_barrier(0);
#pragma unroll
            for (int k = 0; k < 8; ++k)
#pragma unroll
                for (int m = 0; m < 2; ++m)
#pragma unroll
                    for (int n = 0; n < 2; ++n) acc[m][n] = mfma16(B[bt * 8 + k][n], A[bt % 3][k][m], acc[m][n]);
            asm volatile("" : "+v"(acc[0][0]), "+v"(acc[0][1]), "+v"(acc[1][0]), "+v"(acc[1][1]));
            __builtin_amdgcn_sched_barrier(0);
        }
        v4f r = ksplit_reduce(acc, smem, wave, lane);
        float r0 = fmaf(ga[0], r[0], lo16f(xv.x)), r1 = fmaf(ga[1], r[1], hi16f(xv.x));
        float r2 = fmaf(ga[2], r[2], lo16f(xv.y)), r3 = fmaf(ga[3], r[3], hi16f(xv.y));
        v2u o; o.x = pack2(r0, r1); o.y = pack2(r2, r3);
        bst2<16>(rs, (unsigned)(WS_X1 + ((size_t)row * 512 + c) * 2), o);
        signal_counter(p, CTL_WOC + seg * 16);
    }
}

DEV void worker_mlp1(const Ptrs& p, int grp, int j, int wave, int lane, char* smem) {
    __amdgpu_buffer_rsrc_t rs = mkrs(p.ws);
    v4u B[16][2];
    const int nt0 = j * 8 + wave * 2;
#pragma unroll
    for (int ks = 0; ks < 16; ++ks)
#pragma unroll
        for (int n = 0; n < 2; ++n) B[ks][n] = bld4<0>(rs, (unsigned)(WS_W1 + (size_t)ks * 112 * 1024 + (nt0 + n) * 1024 + lane * 16));
    const int g = lane >> 4, l15 = lane & 15;
    v2u bv[2];
#pragma unroll
    for (int n = 0; n < 2; ++n) bv[n] = *(const v2u*)(p.b1 + (nt0 + n) * 16 + 4 * g);
    const float* mod = (const float*)(p.ws + WS_MOD);
    float sb[8], cb1[8];
#pragma unroll
    for (int i = 0; i < 8; ++i) { sb[i] = mod[1536 + lane * 8 + i]; cb1[i] = 1.f + mod[2048 + lane * 8 + i]; }
    for (int seg = grp; seg < NSEG; seg += 4) {
        wait_counter(p, CTL_WOC + seg * 16, 16, wave, seg >= NSEG - 4);
        v4u xr[8];
#pragma unroll
        for (int i = 0; i < 8; ++i) xr[i] = bld4<16>(rs, (unsigned)(WS_X1 + (size_t)(seg * 32 + wave * 8 + ((i + j) & 7)) * 1024 + lane * 16));
#pragma unroll
        for (int i = 0; i < 8; ++i) {
            float v[8];
            unpack8(xr[i], v);
            ln_row1(v);
#pragma unroll
            for (int q = 0; q < 8; ++q) v[q] = fmaf(v[q], cb1[q], sb[q]);
            ln_row1(v);
            const int r = wave * 8 + ((i + j) & 7);
            *(v4u*)(smem + ((r >> 4) * 16 + (lane >> 2)) * 1024 + ((r & 15) + 16 * (lane & 3)) * 16) = pack8(v);
        }
        BAR_ONLY();
        v4f acc[2][2] = {{{0, 0, 0, 0}, {0, 0, 0, 0}}, {{0, 0, 0, 0}, {0, 0, 0, 0}}};
#pragma unroll
        for (int ks = 0; ks < 16; ++ks) {
            v4u a0 = *(const v4u*)(smem + ks * 1024 + lane * 16), a1 = *(const v4u*)(smem + 16384 + ks * 1024 + lane * 16);
#pragma unroll
            for (int n = 0; n < 2; ++n) { acc[0][n] = mfma16(B[ks][n], a0, acc[0][n]); acc[1][n] = mfma16(B[ks][n], a1, acc[1][n]); }
        }
#pragma unroll
        for (int m = 0; m < 2; ++m) {
            unsigned pk[2][2];
#pragma unroll
            for (int n = 0; n < 2; ++n) {
                const float b0 = lo16f(bv[n].x), b1 = hi16f(bv[n].x), b2 = lo16f(bv[n].y), b3 = hi16f(bv[n].y);
                pk[n][0] = pack2(gelu_tanh(acc[m][n][0] + b0), gelu_tanh(acc[m][n][1] + b1));
                pk[n][1] = pack2(gelu_tanh(acc[m][n][2] + b2), gelu_tanh(acc[m][n][3] + b3));
            }
            auto t0 = __builtin_amdgcn_permlane16_swap(pk[0][0], pk[1][0], false, false);
            auto t1 = __builtin_amdgcn_permlane16_swap(pk[0][1], pk[1][1], false, false);
            v4u ov = {t0[0], t1[0], t0[1], t1[1]};
            bst4<16>(rs, (unsigned)(WS_G + ((size_t)(seg * 2 + m) * 56 + (nt0 >> 1)) * 1024 + (l15 + 16 * (2 * (g & 1) + (g >> 1))) * 16), ov);
        }
        signal_counter(p, CTL_M1C + seg * 16);
    }
}

DEV void worker_mlp2(const Ptrs& p, int grp, int j, int wave, int lane, char* smem) {
    __amdgpu_buffer_rsrc_t rs = mkrs(p.ws);
    v4u B[14][2];
#pragma unroll
    for (int ks = 0; ks < 14; ++ks)
#pragma unroll
        for (int n = 0; n < 2; ++n) B[ks][n] = bld4<0>(rs, (unsigned)(WS_W2 + (size_t)(wave * 14 + (ks + j) % 14) * 32768 + (2 * j + n) * 1024 + lane * 16));
    const int g = lane >> 4, l15 = lane & 15;
    const int c = (2 * j + (wave & 1)) * 16 + 4 * g;
    const v4f gb = *(const v4f*)((const float*)(p.ws + WS_MOD) + 2560 + c);
    const v2u bv = *(const v2u*)(p.b2 + c);
    const float b0 = lo16f(bv.x), b1 = hi16f(bv.x), b2 = lo16f(bv.y), b3 = hi16f(bv.y);
    for (int seg = grp; seg < NSEG; seg += 4) {
        const int row = (seg * 2 + (wave >> 1)) * 16 + l15;
        wait_counter(p, CTL_M1C + seg * 16, 14, wave, seg >= NSEG - 4);
        const v2u xv = bld2<16>(rs, (unsigned)(WS_X1 + ((size_t)row * 512 + c) * 2));
        v4f acc[2][2] = {{{0, 0, 0, 0}, {0, 0, 0, 0}}, {{0, 0, 0, 0}, {0, 0, 0, 0}}};
        const unsigned aoff = (unsigned)(WS_G + (size_t)seg * 2 * 57344 + wave * 14 * 1024 + lane * 16);
        v4u A[14][2];
#pragma unroll
        for (int k = 0; k < 14; ++k) { const unsigned ko = (unsigned)((k + j) % 14) * 1024; A[k][0] = bld4<16>(rs, aoff + ko); A[k][1] = bld4<16>(rs, aoff + 57344 + ko); }
#pragma unroll
        for (int k = 0; k < 14; ++k)
#pragma unroll
            for (int m = 0; m < 2; ++m)
#pragma unroll
                for (int n = 0; n < 2; ++n) acc[m][n] = mfma16(B[k][n], A[k][m], acc[m][n]);
        v4f r = ksplit_reduce(acc, smem, wave, lane);
        float r0 = fmaf(gb[0], r[0] + b0, lo16f(xv.x)), r1 = fmaf(gb[1], r[1] + b1, hi16f(xv.x));
        float r2 = fmaf(gb[2], r[2] + b2, lo16f(xv.y)), r3 = fmaf(gb[3], r[3] + b3, hi16f(xv.y));
        v2u o; o.x = pack2(r0, r1); o.y = pack2(r2, r3);
        *(v2u*)(p.out + (size_t)row * 512 + c) = o;
        BAR_ONLY();
    }
}

__global__ void __launch_bounds__(256, 1) k_mega(Ptrs p) {
    extern __shared__ __attribute__((aligned(16))) char smem[];
    const int b = blockIdx.x;
    const int x = b & 7, i = b >> 3, grp = x >> 1;
    if ((x & 1) && i < 16) { head_main(p, (i >> 3) * 32 + grp * 8 + (i & 7), smem); return; }
    const int wave = __builtin_amdgcn_readfirstlane(threadIdx.x >> 6), lane = threadIdx.x & 63;
    const int u = (x & 1) ? 32 + (i - 16) : i;
    const int w = u * 4 + grp;
    if (w < 136) ln1_block(p, w, smem);
    for (int n = u; n < 256; n += 48) {
        const int rb = n >> 3, hh = grp * 8 + (n & 7);
        wait_counter(p, CTL_LNC + rb * 16, 4, wave, true);
        task_qkv<16>(p, rb, hh, wave, lane);
        DRAIN();
        BAR_ONLY();
        if (threadIdx.x == 0) st_ctl(p, CTL_QKVF + rb * 32 + hh, 1u);
    }
    if (u < 16) worker_wo(p, grp, u, wave, lane, smem);
    else if (u < 32) worker_mlp2(p, grp, u - 16, wave, lane, smem);
    else if (u < 46) worker_mlp1(p, grp, u - 32, wave, lane, smem);
}

extern "C" void kernel_launch(void* const* d_in, const int* in_sizes, int n_in, void* d_out, int out_size, void* d_ws, size_t ws_size, hipStream_t stream) {
    Ptrs p;
    p.x = (const bf16_t*)d_in[0]; p.mem0 = (const bf16_t*)d_in[1]; p.wq = (const bf16_t*)d_in[2]; p.wk = (const bf16_t*)d_in[3];
    p.wv = (const bf16_t*)d_in[4]; p.wo = (const bf16_t*)d_in[5]; p.w1 = (const bf16_t*)d_in[8]; p.b1 = (const bf16_t*)d_in[9];
    p.w2 = (const bf16_t*)d_in[10]; p.b2 = (const bf16_t*)d_in[11]; p.c = (const bf16_t*)d_in[12]; p.wmod = (const bf16_t*)d_in[13];
    p.ws = (char*)d_ws; p.out = (bf16_t*)d_out;
    (void)hipFuncSetAttribute((const void*)k_mega, hipFuncAttributeMaxDynamicSharedMemorySize, HL_END);
    k_prep<<<PREP_BLOCKS, 256, 0, stream>>>(p);
    k_mega<<<256, 256, HL_END, stream>>>(p);
}
```
